```python
import torch

def _softmax(x, axis=-1):
    return torch.softmax(x, dim=axis)
def _gelu(x):
    return torch.nn.functional.gelu(x, approximate="tanh")
def _silu(x):
    return torch.nn.functional.silu(x)
def _sigmoid(x):
    return torch.sigmoid(x)
def _layernorm(x, axis=-1, eps=1e-5):
    m = x.mean(dim=axis, keepdim=True)
    v = x.var(dim=axis, keepdim=True, unbiased=False)
    return (x - m) / torch.sqrt(v + eps)
def _scatter_add(tgt, idx, src, axis=0):
    return tgt.clone().index_add_(axis, idx, src)
def _segment_sum(src, seg, n):
    out = torch.zeros((n,) + src.shape[1:], dtype=src.dtype)
    return out.index_add_(0, seg, src)
def _pad(x, pw):
    flat = tuple(v for pair in reversed(pw) for v in pair)
    return torch.nn.functional.pad(x, flat)

kernel_name = 'parallel_sum__gqa_block_cached__glu_gated_bilinear__decode'

def setup_inputs():

    _w = (lambda a: (lambda t: t.to(torch.bfloat16) if t.is_floating_point() else t)(torch.as_tensor(a)))
    def _A_setup():
        B, Sq, D = 4, 1, 512
        Hq, Hkv, Sk, Dh = 32, 8, 4096, 128
        assert Hq % Hkv == 0, (Hq, Hkv)
        return {
            "x": _w((lambda *s: torch.randn(*s, dtype=torch.bfloat16) * 0.05)(B, Sq, D)),
            "k_cache": _w((lambda *s: torch.randn(*s, dtype=torch.bfloat16) * 1.6)(B, Hkv, Sk, Dh)),
            "v_cache": _w((lambda *s: torch.randn(*s, dtype=torch.bfloat16) * 0.8)(B, Hkv, Sk, Dh)),
            "wq": _w((lambda *s: torch.randn(*s, dtype=torch.bfloat16) * 1.6)(D, Hq * Dh) * (D ** -0.5)),
            "wo": _w((lambda *s: torch.randn(*s, dtype=torch.bfloat16) * 0.8)(Hq * Dh, D) * ((Hq * Dh) ** -0.5)),
        }
    _A_in = _A_setup()

    def _B_setup():
        B, S, D, F = 4, 1, 512, 1792
        return {
            "x": _w((lambda *s: torch.randn(*s, dtype=torch.bfloat16) * 0.05)(B, S, D)),
            "w_gate": _w((lambda *s: torch.randn(*s, dtype=torch.bfloat16) * 0.4)(D, F) * (D ** -0.5)),
            "w_up": _w((lambda *s: torch.randn(*s, dtype=torch.bfloat16) * 0.4)(D, F) * (D ** -0.5)),
            "w_down": _w((lambda *s: torch.randn(*s, dtype=torch.bfloat16) * 0.4)(F, D) * (F ** -0.5)),
        }
    _B_in = _B_setup()

    return {
        "A_x": _A_in["x"],
        "A_k_cache": _A_in["k_cache"],
        "A_v_cache": _A_in["v_cache"],
        "A_wq": _A_in["wq"],
        "A_wo": _A_in["wo"],
        "B_x": _B_in["x"],
        "B_w_gate": _B_in["w_gate"],
        "B_w_up": _B_in["w_up"],
        "B_w_down": _B_in["w_down"],
    }

def reference(A_x, A_k_cache, A_v_cache, A_wq, A_wo, B_x, B_w_gate, B_w_up, B_w_down):
    def _A_ref(x, k_cache, v_cache, wq, wo):
        B, Sq, D = x.shape
        Hq, Dh = 32, 128
        Hkv = k_cache.shape[1]
        rep = Hq // Hkv
        q = (lambda x, *d: x.permute(*d))((x @ wq).reshape(B, Sq, Hq, Dh), 0, 2, 1, 3)
        k = (lambda x, n, axis: torch.repeat_interleave(x, n, dim=axis))(k_cache, rep, axis=1)
        v = (lambda x, n, axis: torch.repeat_interleave(x, n, dim=axis))(v_cache, rep, axis=1)
        scores = torch.einsum("bhqd,bhkd->bhqk", q, k) * (Dh ** -0.5)
        p = _softmax(scores, axis=-1)
        o = torch.einsum("bhqk,bhkd->bhqd", p, v)
        return (lambda x, *d: x.permute(*d))(o, 0, 2, 1, 3).reshape(B, Sq, Hq * Dh) @ wo

    def _B_ref(x, w_gate, w_up, w_down):
        g = torch.einsum("bsd,df->bsf", x, w_gate)
        u = torch.einsum("bsd,df->bsf", x, w_up)
        h = g * u
        return torch.einsum("bsf,fd->bsd", h, w_down)

    x = A_x
    xn = _layernorm(x, axis=-1)
    return x + _A_ref(xn, A_k_cache, A_v_cache, A_wq, A_wo) + _B_ref(xn, B_w_gate, B_w_up, B_w_down)
```

```cpp
#include <hip/hip_runtime.h>
#include <hip/hip_bf16.h>
#include <stdint.h>

typedef unsigned v4u __attribute__((ext_vector_type(4)));
typedef float f32x4 __attribute__((ext_vector_type(4)));
typedef __bf16 bf16x2 __attribute__((ext_vector_type(2)));

constexpr int B = 4, D = 512, HQ = 32, HKV = 8, SK = 4096, DH = 128, F = 1792;
constexpr int NSPLIT = 8;

__device__ __forceinline__ float bflo(unsigned u) { return __builtin_bit_cast(float, u << 16); }
__device__ __forceinline__ float bfhi(unsigned u) { return __builtin_bit_cast(float, u & 0xffff0000u); }
__device__ __forceinline__ float bf2f(unsigned short u) { return __builtin_bit_cast(float, (unsigned)u << 16); }
__device__ __forceinline__ float rbf(float f) { return (float)(__bf16)f; }
__device__ __forceinline__ unsigned short f2bf(float f) { __bf16 b = (__bf16)f; return __builtin_bit_cast(unsigned short, b); }

__device__ __forceinline__ void layernorm_lds(const unsigned short* x, float* xn, int tid) {
  int wave = tid >> 6, lane = tid & 63;
  if (wave < 4) {
    v4u v = *(const v4u*)(x + wave * D + lane * 8);
    float e[8];
    for (int i = 0; i < 4; ++i) { e[2 * i] = bflo(v[i]); e[2 * i + 1] = bfhi(v[i]); }
    float s = 0.f;
    for (int i = 0; i < 8; ++i) s += e[i];
    for (int o = 32; o >= 1; o >>= 1) s += __shfl_xor(s, o);
    float mean = s * (1.f / D);
    float ss = 0.f;
    for (int i = 0; i < 8; ++i) { float d = e[i] - mean; ss += d * d; }
    for (int o = 32; o >= 1; o >>= 1) ss += __shfl_xor(ss, o);
    float var = rbf(ss * (1.f / D));
    float m = rbf(mean);
    float den = rbf(sqrtf(rbf(var + 1e-5f)));
    for (int i = 0; i < 8; ++i) xn[wave * D + lane * 8 + i] = rbf(rbf(e[i] - m) / den);
  }
}

__global__ void __launch_bounds__(256) k1(const unsigned short* x, const unsigned short* wq, const unsigned short* wg, const unsigned short* wu, float* q, float* h) {
  __shared__ float xn[4 * D];
  __shared__ float red[2][32][4][64];
  int tid = threadIdx.x;
  layernorm_lds(x, xn, tid);
  __syncthreads();
  int blk = blockIdx.x;
  int cg = tid & 7, rg = tid >> 3;
  if (blk < 64) {
    float acc[4][8] = {};
    for (int r = rg; r < D; r += 32) {
      v4u w = *(const v4u*)(wq + (size_t)r * 4096 + blk * 64 + cg * 8);
      for (int b = 0; b < 4; ++b) { float xv = xn[b * D + r];
        for (int i = 0; i < 4; ++i) { acc[b][2 * i] += xv * bflo(w[i]); acc[b][2 * i + 1] += xv * bfhi(w[i]); } }
    }
    for (int b = 0; b < 4; ++b) for (int i = 0; i < 8; ++i) red[0][rg][b][cg * 8 + i] = acc[b][i];
    __syncthreads();
    int b = tid >> 6, c = tid & 63; float s = 0.f;
    for (int g = 0; g < 32; ++g) s += red[0][g][b][c];
    q[b * 4096 + blk * 64 + c] = rbf(s);
  } else {
    int cb = blk - 64;
    float ag[4][8] = {}, au[4][8] = {};
    for (int r = rg; r < D; r += 32) {
      v4u w = *(const v4u*)(wg + (size_t)r * F + cb * 64 + cg * 8);
      v4u u = *(const v4u*)(wu + (size_t)r * F + cb * 64 + cg * 8);
      for (int b = 0; b < 4; ++b) { float xv = xn[b * D + r];
        for (int i = 0; i < 4; ++i) { ag[b][2 * i] += xv * bflo(w[i]); ag[b][2 * i + 1] += xv * bfhi(w[i]); au[b][2 * i] += xv * bflo(u[i]); au[b][2 * i + 1] += xv * bfhi(u[i]); } }
    }
    for (int b = 0; b < 4; ++b) for (int i = 0; i < 8; ++i) { red[0][rg][b][cg * 8 + i] = ag[b][i]; red[1][rg][b][cg * 8 + i] = au[b][i]; }
    __syncthreads();
    int b = tid >> 6, c = tid & 63; float s = 0.f, t = 0.f;
    for (int g = 0; g < 32; ++g) { s += red[0][g][b][c]; t += red[1][g][b][c]; }
    h[b * F + cb * 64 + c] = rbf(rbf(s) * rbf(t));
  }
}

__global__ void __launch_bounds__(256) k2(const float* q, const unsigned short* K, const unsigned short* V, float* po, float* pml) {
  __shared__ float sc[4][512];
  __shared__ float red[16][4][128];
  __shared__ float mx[4], sm[4];
  int s = blockIdx.x, kvh = blockIdx.y, b = blockIdx.z;
  int tid = threadIdx.x, c = tid & 15, r = tid >> 4;
  float qv[4][8];
  for (int g = 0; g < 4; ++g) for (int i = 0; i < 8; ++i) qv[g][i] = q[b * 4096 + (kvh * 4 + g) * 128 + c * 8 + i];
  const unsigned short* kb = K + ((size_t)(b * HKV + kvh) * SK + s * 512) * DH;
  const unsigned short* vb = V + ((size_t)(b * HKV + kvh) * SK + s * 512) * DH;
#pragma unroll 8
  for (int t = r; t < 512; t += 16) {
    v4u k = __builtin_nontemporal_load((const v4u*)(kb + t * DH + c * 8));
    float d[4];
    for (int g = 0; g < 4; ++g) { float a = 0.f; for (int i = 0; i < 4; ++i) { a += qv[g][2 * i] * bflo(k[i]) + qv[g][2 * i + 1] * bfhi(k[i]); } d[g] = a; }
    for (int g = 0; g < 4; ++g) { for (int o = 8; o >= 1; o >>= 1) d[g] += __shfl_xor(d[g], o); }
    if (c < 4) sc[c][t] = (c == 0 ? d[0] : c == 1 ? d[1] : c == 2 ? d[2] : d[3]) * 0.08838834764831845f;
  }
  __syncthreads();
  {
    int g = tid >> 6, lane = tid & 63;
    float m = -INFINITY;
    for (int t = lane; t < 512; t += 64) m = fmaxf(m, sc[g][t]);
    for (int o = 32; o >= 1; o >>= 1) m = fmaxf(m, __shfl_xor(m, o));
    float l = 0.f;
    for (int t = lane; t < 512; t += 64) { float p = __expf(sc[g][t] - m); sc[g][t] = p; l += p; }
    for (int o = 32; o >= 1; o >>= 1) l += __shfl_xor(l, o);
    if (lane == 0) { mx[g] = m; sm[g] = l; }
  }
  __syncthreads();
  float acc[4][8] = {};
#pragma unroll 8
  for (int t = r; t < 512; t += 16) {
    v4u v = __builtin_nontemporal_load((const v4u*)(vb + t * DH + c * 8));
    for (int g = 0; g < 4; ++g) { float p = sc[g][t]; for (int i = 0; i < 4; ++i) { acc[g][2 * i] += p * bflo(v[i]); acc[g][2 * i + 1] += p * bfhi(v[i]); } }
  }
  for (int g = 0; g < 4; ++g) for (int i = 0; i < 8; ++i) red[r][g][c * 8 + i] = acc[g][i];
  __syncthreads();
  for (int o = tid; o < 512; o += 256) {
    int g = o >> 7, d = o & 127; float a = 0.f;
    for (int rr = 0; rr < 16; ++rr) a += red[rr][g][d];
    po[((size_t)(b * HQ + kvh * 4 + g) * NSPLIT + s) * DH + d] = a;
  }
  if (tid < 4) { pml[((b * HQ + kvh * 4 + tid) * NSPLIT + s) * 2] = mx[tid]; pml[((b * HQ + kvh * 4 + tid) * NSPLIT + s) * 2 + 1] = sm[tid]; }
}

__global__ void __launch_bounds__(256) k3a(const float* po, const float* pml, const float* h, const unsigned short* wo, const unsigned short* wd, float* part) {
  __shared__ float a[4][128];
  __shared__ float red[4][4][512];
  int tid = threadIdx.x, blk = blockIdx.x;
  const unsigned short* w;
  if (blk < 32) {
    for (int o = tid; o < 512; o += 256) {
      int b = o >> 7, d = o & 127;
      const float* ml = pml + (b * HQ + blk) * NSPLIT * 2;
      float M = -INFINITY; for (int s = 0; s < NSPLIT; ++s) M = fmaxf(M, ml[2 * s]);
      float L = 0.f, o_ = 0.f;
      for (int s = 0; s < NSPLIT; ++s) { float wt = __expf(ml[2 * s] - M); L += wt * ml[2 * s + 1]; o_ += wt * po[((size_t)(b * HQ + blk) * NSPLIT + s) * DH + d]; }
      a[b][d] = rbf(o_ / L);
    }
    w = wo + (size_t)blk * 128 * D;
  } else {
    for (int o = tid; o < 512; o += 256) { int b = o >> 7, d = o & 127; a[b][d] = h[b * F + (blk - 32) * 128 + d]; }
    w = wd + (size_t)(blk - 32) * 128 * D;
  }
  __syncthreads();
  int cg = tid & 63, rg = tid >> 6;
  float acc[4][8] = {};
#pragma unroll 8
  for (int r = rg; r < 128; r += 4) {
    v4u ww = *(const v4u*)(w + (size_t)r * D + cg * 8);
    for (int b = 0; b < 4; ++b) { float xv = a[b][r]; for (int i = 0; i < 4; ++i) { acc[b][2 * i] += xv * bflo(ww[i]); acc[b][2 * i + 1] += xv * bfhi(ww[i]); } }
  }
  for (int b = 0; b < 4; ++b) for (int i = 0; i < 8; ++i) red[rg][b][cg * 8 + i] = acc[b][i];
  __syncthreads();
  for (int o = tid; o < 2048; o += 256) { int b = o >> 9, c = o & 511; part[blk * 2048 + o] = red[0][b][c] + red[1][b][c] + red[2][b][c] + red[3][b][c]; }
}

__global__ void __launch_bounds__(256) k3b(const float* part, const unsigned short* x, unsigned short* out) {
  int o = blockIdx.x * 256 + threadIdx.x;
  float A = 0.f, Bv = 0.f;
  for (int i = 0; i < 32; ++i) A += part[i * 2048 + o];
  for (int i = 32; i < 46; ++i) Bv += part[i * 2048 + o];
  out[o] = f2bf(rbf(bf2f(x[o]) + rbf(A)) + rbf(Bv));
}

extern "C" void kernel_launch(void* const* d_in, const int* in_sizes, int n_in, void* d_out, int out_size, void* d_ws, size_t ws_size, hipStream_t stream) {
  const unsigned short* x = (const unsigned short*)d_in[0];
  const unsigned short* K = (const unsigned short*)d_in[1];
  const unsigned short* V = (const unsigned short*)d_in[2];
  const unsigned short* wq = (const unsigned short*)d_in[3];
  const unsigned short* wo = (const unsigned short*)d_in[4];
  const unsigned short* wg = (const unsigned short*)d_in[6];
  const unsigned short* wu = (const unsigned short*)d_in[7];
  const unsigned short* wd = (const unsigned short*)d_in[8];
  float* ws = (float*)d_ws;
  float* q = ws;
  float* h = ws + 16384;
  float* pml = ws + 32768;
  float* po = ws + 65536;
  float* part = ws + 262144;
  k1<<<92, 256, 0, stream>>>(x, wq, wg, wu, q, h);
  k2<<<dim3(NSPLIT, HKV, B), 256, 0, stream>>>(q, K, V, po, pml);
  k3a<<<46, 256, 0, stream>>>(po, pml, h, wo, wd, part);
  k3b<<<8, 256, 0, stream>>>(part, x, (unsigned short*)d_out);
}
```

```cpp
#include <hip/hip_runtime.h>
#include <hip/hip_bf16.h>
#include <stdint.h>

#define LAS __attribute__((address_space(3)))
#define GAS __attribute__((address_space(1)))
typedef unsigned v4u __attribute__((ext_vector_type(4)));
typedef unsigned v2u __attribute__((ext_vector_type(2)));
typedef float f32x4 __attribute__((ext_vector_type(4)));
typedef short s16x4 __attribute__((ext_vector_type(4)));
typedef __bf16 bf16x8 __attribute__((ext_vector_type(8)));
typedef __bf16 bf16x2 __attribute__((ext_vector_type(2)));
typedef float f32x2 __attribute__((ext_vector_type(2)));
typedef unsigned long long u64;

#define EMU_ROUND 1
#define NFL 8
#define QTRIG 18
#define STAMP_BLOCKS 256
#define NSTAMP 16

constexpr int NWG = 256, NTHR = 512;
constexpr int AUX_NT = 2, AUX_SC1 = 16, RSRC_W3 = 0x00020000;
constexpr int WS_EPOCH = 0, WS_FLAG = 2048, WS_ML = 4096, WS_QP = WS_ML + 4 * 32 * 64, WS_GP = WS_QP + 4 * 4 * 4096 * 8,
              WS_O = WS_GP + 2 * 4 * 4 * 1792 * 4, WS_P2 = WS_O + 4 * 32 * 8 * 512, WS_END = WS_P2 + 8 * 32 * 1024;
constexpr int L_K = 0, L_WO = 131072, L_WD = L_WO + 16384, L_QS = L_WD + 8192, L_OS = L_QS + 1280, L_HS = L_OS + 1280, L_XS = L_HS + 768,
              L_MX = L_XS + 2176, L_LX = L_MX + 64, L_CT = L_LX + 64, L_END = L_CT + 64;
enum { C_QRDY = 0, C_MAX = 1, C_PRDY = 2  , C_PCNT = 6, C_MRG = 7, C_PUB = 8, C_VISS = 9, C_KRDY = 10  , C_QTRIG = 14, C_CERT = 15 };
constexpr unsigned SENT = 0xFFFFFFFFu;
constexpr unsigned GSPIN = 1u << 16, LSPIN = 1u << 19;

__device__ __forceinline__ float bflo(unsigned u) { return __builtin_bit_cast(float, u << 16); }
__device__ __forceinline__ float bfhi(unsigned u) { return __builtin_bit_cast(float, u & 0xffff0000u); }
__device__ __forceinline__ unsigned pk2(float lo, float hi) { f32x2 f = {lo, hi}; return __builtin_bit_cast(unsigned, __builtin_convertvector(f, bf16x2)); }
__device__ __forceinline__ float rbf(float f) { return bflo(pk2(f, f)); }
template <int CTRL> __device__ __forceinline__ float dpp(float x) { return __builtin_bit_cast(float, __builtin_amdgcn_mov_dpp(__builtin_bit_cast(int, x), CTRL, 0xf, 0xf, true)); }
template <int CTRL> __device__ __forceinline__ unsigned dppu(unsigned x) { return (unsigned)__builtin_amdgcn_mov_dpp((int)x, CTRL, 0xf, 0xf, true); }
constexpr int XOR1 = 0xB1, XOR2 = 0x4E, HMIR = 0x141, ROR8 = 0x128, ROR4 = 0x124;
__device__ __forceinline__ float row16_sum(float x) { x += dpp<XOR1>(x); x += dpp<XOR2>(x); x += dpp<HMIR>(x); x += dpp<ROR8>(x); return x; }
__device__ __forceinline__ float x16_sum(float x) { auto s = __builtin_amdgcn_permlane16_swap(__float_as_uint(x), __float_as_uint(x), false, false); return __uint_as_float(s[0]) + __uint_as_float(s[1]); }
__device__ __forceinline__ float x32_sum(float x) { auto s = __builtin_amdgcn_permlane32_swap(__float_as_uint(x), __float_as_uint(x), false, false); return __uint_as_float(s[0]) + __uint_as_float(s[1]); }
__device__ __forceinline__ float x16_max(float x) { auto s = __builtin_amdgcn_permlane16_swap(__float_as_uint(x), __float_as_uint(x), false, false); return fmaxf(__uint_as_float(s[0]), __uint_as_float(s[1])); }
__device__ __forceinline__ float x32_max(float x) { auto s = __builtin_amdgcn_permlane32_swap(__float_as_uint(x), __float_as_uint(x), false, false); return fmaxf(__uint_as_float(s[0]), __uint_as_float(s[1])); }
__device__ __forceinline__ f32x4 mfma16(v2u a, v2u b, f32x4 c) { return __builtin_amdgcn_mfma_f32_16x16x16bf16_1k(__builtin_bit_cast(s16x4, a), __builtin_bit_cast(s16x4, b), c, 0, 0, 0); }
__device__ __forceinline__ f32x4 mfma32(v4u a, v4u b, f32x4 c) { return __builtin_amdgcn_mfma_f32_16x16x32_bf16(__builtin_bit_cast(bf16x8, a), __builtin_bit_cast(bf16x8, b), c, 0, 0, 0); }
__device__ __forceinline__ v4u canon(f32x4 v) { v4u u = __builtin_bit_cast(v4u, v); u.x = min(u.x, SENT - 1); u.y = min(u.y, SENT - 1); u.z = min(u.z, SENT - 1); u.w = min(u.w, SENT - 1); return u; }
__device__ __forceinline__ unsigned umax4(v4u u) { return max(max(u.x, u.y), max(u.z, u.w)); }
__device__ __forceinline__ v2u trrd(LAS char* p) { return __builtin_bit_cast(v2u, __builtin_amdgcn_ds_read_tr16_b64_v4i16((LAS s16x4*)p)); }
#define DRAIN_VM() asm volatile("s_waitcnt vmcnt(0)" ::: "memory")
#define DRAIN_LGKM() asm volatile("s_waitcnt lgkmcnt(0)" ::: "memory")
#define MEMFENCE() asm volatile("" ::: "memory")
#define SB() __builtin_amdgcn_sched_barrier(0)

__device__ __forceinline__ unsigned lds_ld(LAS unsigned* p) { return __hip_atomic_load(p, __ATOMIC_RELAXED, __HIP_MEMORY_SCOPE_WORKGROUP); }
__device__ __forceinline__ void lds_st(LAS unsigned* p, unsigned v) { __hip_atomic_store(p, v, __ATOMIC_RELAXED, __HIP_MEMORY_SCOPE_WORKGROUP); }
__device__ __forceinline__ void lds_wait_ge(LAS unsigned* p, unsigned want) {
#pragma unroll 1
  for (unsigned n = 0; n < LSPIN; ++n) {
    if ((unsigned)__builtin_amdgcn_readfirstlane(lds_ld(p)) >= want) break;
    __builtin_amdgcn_s_sleep(1);
  }
  MEMFENCE();
}
__device__ __forceinline__ void lds_add1(LAS unsigned* p, int lane) {
  if (lane == 0) asm volatile("ds_add_u32 %0, %1" :: "v"((unsigned)(size_t)p), "v"(1u) : "memory");
}
#define STAMP(k) do {} while (0)
#define LDS_EXTRA 0

__device__ __forceinline__ int pos2col(int pos) { return 32 * ((pos >> 4) & 1) + 8 * (pos & 3) + 4 * (pos >> 5) + ((pos >> 2) & 3); }

__device__ __forceinline__ f32x4 ln_tile(const v4u (&xr)[4], const v4u (&w)[16], LAS char* xs, int lane) {
  float e[32];
#pragma unroll
  for (int k = 0; k < 4; ++k)
#pragma unroll
    for (int j = 0; j < 4; ++j) { e[8 * k + 2 * j] = bflo(xr[k][j]); e[8 * k + 2 * j + 1] = bfhi(xr[k][j]); }
  float s0 = 0.f, s1 = 0.f;
#pragma unroll
  for (int k = 0; k < 16; ++k) { s0 += e[2 * k]; s1 += e[2 * k + 1]; }
  const float mean = row16_sum(s0 + s1) * (1.f / 512.f);
  float q0 = 0.f, q1 = 0.f;
#pragma unroll
  for (int k = 0; k < 16; ++k) { float d0 = e[2 * k] - mean, d1 = e[2 * k + 1] - mean; q0 = fmaf(d0, d0, q0); q1 = fmaf(d1, d1, q1); }
  const float var = rbf(row16_sum(q0 + q1) * (1.f / 512.f));
  const float mb = rbf(mean), den = rbf(sqrtf(rbf(var + 1e-5f))), inv = 1.f / den;
  v4u xo;
#pragma unroll
  for (int j = 0; j < 4; ++j) xo[j] = pk2(rbf(e[2 * j] - mb) * inv, rbf(e[2 * j + 1] - mb) * inv);
  *(LAS v4u*)(xs + (lane >> 4) * 272 + (lane & 15) * 16) = xo;
  DRAIN_LGKM();
  const int g = lane >> 4, bb = (lane >> 2) & 3, jj = lane & 3;
  unsigned pr[16];
#pragma unroll
  for (int i = 0; i < 16; ++i) pr[i] = *(LAS unsigned*)(xs + bb * 272 + (8 * i + 2 * g) * 2);
  SB();
  const unsigned z = 0x0c0c0c0cu;
  const unsigned sA0 = jj == 0 ? 0x0c0c0100u : jj == 1 ? 0x01000c0cu : z, sB0 = jj == 2 ? 0x0c0c0100u : jj == 3 ? 0x01000c0cu : z;
  const unsigned sA1 = jj == 0 ? 0x0c0c0302u : jj == 1 ? 0x03020c0cu : z, sB1 = jj == 2 ? 0x0c0c0302u : jj == 3 ? 0x03020c0cu : z;
  f32x4 d00 = {0, 0, 0, 0}, d01 = d00, d10 = d00, d11 = d00;
#pragma unroll
  for (int i = 0; i < 16; ++i) {
    const v2u b0 = {__builtin_amdgcn_perm(pr[i], pr[i], sA0), __builtin_amdgcn_perm(pr[i], pr[i], sB0)};
    const v2u b1 = {__builtin_amdgcn_perm(pr[i], pr[i], sA1), __builtin_amdgcn_perm(pr[i], pr[i], sB1)};
    const v2u lo = {w[i].x, w[i].y}, hi = {w[i].z, w[i].w};
    d00 = mfma16(lo, b0, d00); d01 = mfma16(hi, b0, d01); d10 = mfma16(lo, b1, d10); d11 = mfma16(hi, b1, d11);
  }
  f32x4 y;
#pragma unroll
  for (int t = 0; t < 4; ++t) {
    auto r = __builtin_amdgcn_permlane32_swap(__float_as_uint(d10[t]), __float_as_uint(d01[t]), false, false);
    y[t] = lane < 32 ? d00[t] + __uint_as_float(r[1]) : d11[t] + __uint_as_float(r[0]);
  }
  return y;
}

__global__ void __launch_bounds__(NTHR, 2)
fused_decode(const char* __restrict__ x, const char* __restrict__ Kc, const char* __restrict__ Vc, const char* __restrict__ wq, const char* __restrict__ wo,
             const char* __restrict__ wg, const char* __restrict__ wu, const char* __restrict__ wd, unsigned short* __restrict__ out, char* ws) {
  __shared__ __attribute__((aligned(16))) char smem_[L_END + LDS_EXTRA];
  LAS char* L = (LAS char*)smem_;
  LAS unsigned* ct = (LAS unsigned*)(L + L_CT);
  const int lane = threadIdx.x & 63, wave = __builtin_amdgcn_readfirstlane(threadIdx.x >> 6), blk = blockIdx.x;
  const int grp = blk & 7, mem = blk >> 3, hi2 = mem >> 3, lo3 = mem & 7;
  const int hh = 4 * grp + hi2;
  asm volatile("" :: "s"(x), "s"(Kc), "s"(Vc), "s"(wq), "s"(wo), "s"(wg), "s"(wu), "s"(wd), "s"(out), "s"(ws));
  const __amdgpu_buffer_rsrc_t rw = __builtin_amdgcn_make_buffer_rsrc((void*)ws, 0, WS_END, RSRC_W3);
  const int r = lane >> 4, c = lane & 15, hd = (lane >> 2) & 3, jj = lane & 3;

  if (wave >= 4) {
    const int sw = wave - 4;
    if (sw == 0) STAMP(0);
    const __amdgpu_buffer_rsrc_t rk = __builtin_amdgcn_make_buffer_rsrc((void*)(Kc + (size_t)(hi2 * 8 + grp) * 1048576), 0, 1048576, RSRC_W3);
    const __amdgpu_buffer_rsrc_t rv = __builtin_amdgcn_make_buffer_rsrc((void*)(Vc + (size_t)(hi2 * 8 + grp) * 1048576), 0, 1048576, RSRC_W3);
    LAS char* kb = L + L_K + sw * 32768;
    const int sbase = lo3 * 16384 + sw * 1024;
#define THROTTLE() do { asm volatile("s_waitcnt vmcnt(%0)" :: "i"(NFL - 1) : "memory"); SB(); } while (0)
#define KDMA(i) __builtin_amdgcn_raw_ptr_buffer_load_lds(rk, (LAS void*)(kb + (i) * 1024), 16, r * 256 + ((c ^ (4 * r + ((i) & 3))) * 16), sbase + ((i) >> 2) * 131072 + ((i) & 3) * 4096, 0, AUX_NT)
    KDMA(0); KDMA(1);
    SB();
    __builtin_amdgcn_s_barrier();
    SB();
    const int col0 = grp * 64 + mem * 2;
    const unsigned resid_raw = __builtin_amdgcn_raw_buffer_load_b16(__builtin_amdgcn_make_buffer_rsrc((void*)x, 0, 4096, RSRC_W3), (((lane >> 1) & 3) * 512 + col0 + (lane & 1)) * 2, 0, 0);
    const v2u ep = __builtin_amdgcn_raw_buffer_load_b64(rw, WS_EPOCH, 0, AUX_SC1);
    SB();
#pragma unroll
    for (int i = 2; i < 32; ++i) {
      KDMA(i); if (i >= NFL - 1) THROTTLE();
      if (i == QTRIG + NFL - 2 && sw == 0) { if (lane == 0) asm volatile("ds_write_b32 %0, %1" :: "v"((unsigned)(size_t)(ct + C_QTRIG)), "v"(1u) : "memory"); SB(); }
    }
    v4u v[32];
#pragma unroll
    for (int i = 0; i < 32; ++i) {
      v[i] = __builtin_amdgcn_raw_buffer_load_b128(rv, lane * 16, sbase + (i >> 2) * 131072 + (i & 3) * 4096, AUX_NT);
      if (i < 31) THROTTLE();
      if (i == NFL - 2) { if (lane == 0) asm volatile("ds_write_b32 %0, %1" :: "v"((unsigned)(size_t)(ct + C_KRDY + sw)), "v"(1u) : "memory"); if (sw == 0) STAMP(5); SB(); }
    }
    SB();
    lds_add1(ct + C_VISS, lane);
    __builtin_amdgcn_s_setprio(3);
    if (sw == 3) STAMP(8);
    lds_wait_ge(ct + C_PRDY + sw, 1u);
    const v4u P0 = *(LAS v4u*)(kb + lane * 16), P1 = *(LAS v4u*)(kb + 1024 + lane * 16);
    const unsigned mk0 = jj == 0 ? 0x0000ffffu : jj == 1 ? 0xffff0000u : 0u, mk1 = jj == 2 ? 0x0000ffffu : jj == 3 ? 0xffff0000u : 0u;
    f32x4 a0 = {0, 0, 0, 0}, a1 = a0;
#define PV(i) do { const unsigned s_ = ((i) < 16 ? P0 : P1)[(i) & 3]; \
      const v2u p_ = {dppu<0x55 * (((i) >> 2) & 3)>(s_) & mk0, dppu<0x55 * (((i) >> 2) & 3)>(s_) & mk1}; \
      a0 = mfma16(v2u{v[i].x, v[i].y}, p_, a0); a1 = mfma16(v2u{v[i].z, v[i].w}, p_, a1); } while (0)
#define PV4(i) PV(i); PV(i + 1); PV(i + 2); PV(i + 3)
    PV4(0); PV4(4); PV4(8); PV4(12); PV4(16); PV4(20); PV4(24); PV4(28);
    *(LAS f32x4*)(kb + 16384 + lane * 16) = a0; *(LAS f32x4*)(kb + 17408 + lane * 16) = a1;
    unsigned old;
    asm volatile("s_waitcnt lgkmcnt(0)\n\tds_add_rtn_u32 %0, %1, %2\n\ts_waitcnt lgkmcnt(0)" : "=&v"(old) : "v"((unsigned)(size_t)(ct + C_MRG)), "v"(lane == 0 ? 1u : 0u) : "memory");
    old = __builtin_amdgcn_readfirstlane(old);
    if (old == 3u) {
      STAMP(9);
      LAS char* mb = L + L_K + 16384 + lane * 16;
      f32x4 s0 = *(LAS f32x4*)(mb), s1 = *(LAS f32x4*)(mb + 1024);
#pragma unroll
      for (int w = 1; w < 4; ++w) { s0 += *(LAS f32x4*)(mb + w * 32768); s1 += *(LAS f32x4*)(mb + w * 32768 + 1024); }
      const int off = WS_O + ((hi2 * 32 + 4 * grp + hd) * 8 + lo3) * 512 + r * 64 + jj * 16;
      __builtin_amdgcn_raw_buffer_store_b128(canon(s0), rw, off, 0, AUX_SC1);
      __builtin_amdgcn_raw_buffer_store_b128(canon(s1), rw, off + 256, 0, AUX_SC1);
      MEMFENCE();
      if (lane == 0) lds_st(ct + C_PUB, 1u);
      STAMP(10);
    }
    if (sw != 0) return;
    float resid = bflo(resid_raw);
    const u64 tg = (((u64)ep.y << 32) | ep.x) + 1ull;
    v2u tag = v2u{(unsigned)tg, (unsigned)(tg >> 32)};
    asm volatile("" : "+v"(resid), "+v"(tag));
    __builtin_amdgcn_s_barrier();
    MEMFENCE();
  {
    v4u pp;
    const int off = WS_P2 + (grp * 32 + (lane >> 1)) * 1024 + (mem * 2 + (lane & 1)) * 16;
#pragma unroll 1
    for (unsigned n = 0; n < GSPIN; ++n) {
      pp = __builtin_amdgcn_raw_buffer_load_b128(rw, off, 0, AUX_SC1);
      if (__all(umax4(pp) != SENT)) break;
      MEMFENCE();
    }
    STAMP(14);
    const float v0 = __uint_as_float(pp.x), v1 = __uint_as_float(pp.y), v2 = __uint_as_float(pp.z), v3 = __uint_as_float(pp.w);
    const bool b1 = lane & 2, b2 = lane & 4;
    const float e0 = v0 + dpp<XOR2>(v0), e1 = v1 + dpp<XOR2>(v1), e2 = v2 + dpp<XOR2>(v2), e3 = v3 + dpp<XOR2>(v3);
    float f0 = b1 ? e1 : e0, f1 = b1 ? e3 : e2;
    f0 += dpp<ROR4>(f0); f1 += dpp<ROR4>(f1);
    f0 += dpp<ROR8>(f0); f1 += dpp<ROR8>(f1);
    float tot = b2 ? f1 : f0;
    tot = x32_sum(x16_sum(tot)) + resid;
    if (lane < 8) out[(lane >> 1) * 512 + col0 + (lane & 1)] = (unsigned short)(pk2(tot, tot) & 0xffffu);
    if (blk == 0 && lane == 0) __builtin_amdgcn_raw_buffer_store_b64(tag, rw, WS_EPOCH, 0, AUX_SC1);
  }
  STAMP(15);
    return;
  }
  const int kw = wave;
  __builtin_amdgcn_s_setprio(2);
  v2u tag = {0u, 0u};
  if (kw < 2) {
    const __amdgpu_buffer_rsrc_t rx = __builtin_amdgcn_make_buffer_rsrc((void*)x, 0, 4096, RSRC_W3);
    LAS char* xs = L + L_XS + kw * 1088;
    const int which = blk >= 112, tt = blk - 112 * which, rsg = tt / 28, cgg = tt - 28 * rsg;
    const bool isq = kw == 0, have = isq || blk < 224;
    const int rs = isq ? hi2 : rsg, pitch = isq ? 8192 : 3584, cb = isq ? (grp * 512 + lo3 * 64) * 2 : cgg * 128;
    const __amdgpu_buffer_rsrc_t rq = __builtin_amdgcn_make_buffer_rsrc((void*)(isq ? wq : which ? wu : wg), 0, have ? 512 * pitch : 0, RSRC_W3);
    v4u xr[4], w[16];
#pragma unroll
    for (int k = 0; k < 4; ++k) xr[k] = __builtin_amdgcn_raw_buffer_load_b128(rx, r * 1024 + c * 16, ((rs + k) & 3) * 256, 0);
#pragma unroll
    for (int i = 0; i < 16; ++i) w[i] = __builtin_amdgcn_raw_buffer_load_b128(rq, (lane >> 3) * pitch + (lane & 7) * 16, (rs * 128 + 8 * i) * pitch + cb, AUX_NT);
    v2u ep = __builtin_amdgcn_raw_buffer_load_b64(rw, WS_EPOCH, 0, AUX_SC1);
    if (!isq) {
      const v4u sent = {SENT, SENT, SENT, SENT};
      const int offo = WS_O + ((hi2 * 32 + 4 * grp + hd) * 8 + lo3) * 512 + r * 64 + jj * 16;
      __builtin_amdgcn_raw_buffer_store_b128(sent, rw, offo, 0, AUX_SC1);
      __builtin_amdgcn_raw_buffer_store_b128(sent, rw, offo + 256, 0, AUX_SC1);
      __builtin_amdgcn_raw_buffer_store_b128(sent, rw, WS_P2 + (lo3 * 32 + hh) * 1024 + lane * 16, 0, AUX_SC1);
      if (lane < 4) __builtin_amdgcn_raw_buffer_store_b64(v2u{SENT, SENT}, rw, WS_ML + ((hi2 * 32 + 4 * grp + lane) * 8 + lo3) * 8, 0, AUX_SC1);
    }
    SB();
    __builtin_amdgcn_s_barrier();
    SB();
    const f32x4 y = ln_tile(xr, w, xs, lane);
    SB();
    asm volatile("" : "+v"(ep));
    const u64 tg = (((u64)ep.y << 32) | ep.x) + 1ull;
    tag = v2u{(unsigned)tg, (unsigned)(tg >> 32)};
    if (isq) {
      STAMP(1);
      const unsigned t8 = tag.y & 0xffu;
      const int po = WS_QP + ((hi2 * 4 + hd) * 4096 + grp * 512 + lo3 * 64) * 8 + (r * 4 + jj) * 16;
      const v4u yu = __builtin_bit_cast(v4u, y);
      __builtin_amdgcn_raw_buffer_store_b128(v4u{(yu.x & ~0xffu) | t8, tag.x, (yu.y & ~0xffu) | t8, tag.x}, rw, po, 0, AUX_SC1);
      __builtin_amdgcn_raw_buffer_store_b128(v4u{(yu.z & ~0xffu) | t8, tag.x, (yu.w & ~0xffu) | t8, tag.x}, rw, po + 256, 0, AUX_SC1);
      STAMP(2);
      lds_wait_ge(ct + C_QTRIG, 1u);
      STAMP(3);
      v4u g[16];
#pragma unroll 1
      for (unsigned n = 0; n < GSPIN; ++n) {
        unsigned bad = 0;
#pragma unroll
        for (int k = 0; k < 16; ++k) g[k] = __builtin_amdgcn_raw_buffer_load_b128(rw, WS_QP + lane * 16, (((k & 3) * 4 + hi2) * 4096 + grp * 512) * 8 + (k >> 2) * 1024, AUX_SC1);
#pragma unroll
        for (int k = 0; k < 16; ++k) bad |= (g[k].y ^ tag.x) | (g[k].w ^ tag.x) | (((g[k].x ^ t8) | (g[k].z ^ t8)) & 0xffu);
        if (__all(bad == 0)) break;
        MEMFENCE();
        __builtin_amdgcn_s_sleep(8);
      }
#pragma unroll
      for (int u = 0; u < 4; ++u) {
        float s0 = 0.f, s1 = 0.f;
#pragma unroll
        for (int k = 0; k < 4; ++k) { s0 += __uint_as_float(g[4 * u + k].x & ~0xffu); s1 += __uint_as_float(g[4 * u + k].z & ~0xffu); }
        const int col = (2 * u + (r >> 1)) * 64 + pos2col(4 * c) + 16 * (r & 1);
        LAS char* q = L + L_QS + (col >> 7) * 320 + (col & 127) * 2;
        const unsigned a = pk2(s0, s1);
        *(LAS unsigned short*)(q) = (unsigned short)a; *(LAS unsigned short*)(q + 16) = (unsigned short)(a >> 16);
      }
      DRAIN_LGKM();
      if (lane == 0) lds_st(ct + C_QRDY, 1u);
      STAMP(4);
    } else {
      if (have) __builtin_amdgcn_raw_buffer_store_b128(__builtin_bit_cast(v4u, y), rw, WS_GP + (((which * 4 + rsg) * 4 + hd) * 1792 + cgg * 64) * 4 + (r * 4 + jj) * 16, 0, AUX_SC1);
      DRAIN_VM();
      if (lane == 0) __builtin_amdgcn_raw_buffer_store_b64(tag, rw, WS_FLAG + blk * 8, 0, AUX_SC1);
    }
  } else {
    if (kw == 2) { if (lane < 16) ct[lane] = 0u; *(LAS v4u*)(L + L_WD + 7168 + lane * 16) = v4u{0, 0, 0, 0}; DRAIN_LGKM(); }
    __builtin_amdgcn_s_barrier();
  }
  LAS char* kb = L + L_K + kw * 32768;
  lds_wait_ge(ct + C_KRDY + kw, 1u);
  lds_wait_ge(ct + C_QRDY, 1u);
  float sc[8];
  {
    v4u qf[4];
#pragma unroll
    for (int ks = 0; ks < 4; ++ks) qf[ks] = *(LAS v4u*)(L + L_QS + hd * 320 + ks * 64 + r * 16);
    f32x4 S[8];
    LAS char* ka = kb + (4 * (c & 3) + (c >> 2)) * 256;
#pragma unroll
    for (int T = 0; T < 8; ++T) {
      f32x4 a = {0, 0, 0, 0};
#pragma unroll
      for (int ks = 0; ks < 4; ++ks) a = mfma32(*(LAS v4u*)(ka + T * 4096 + (((4 * ks + r) ^ c) * 16)), qf[ks], a);
      S[T] = a;
    }
#pragma unroll
    for (int U = 0; U < 2; ++U)
#pragma unroll
      for (int t = 0; t < 4; ++t) {
        const float lo = (jj & 1) ? S[4 * U + 1][t] : S[4 * U][t], hi = (jj & 1) ? S[4 * U + 3][t] : S[4 * U + 2][t];
        sc[4 * U + t] = (jj & 2) ? hi : lo;
      }
  }
  float mx = -INFINITY;
#pragma unroll
  for (int k = 0; k < 8; k += 2) {
    const unsigned u1 = pk2(sc[k], sc[k + 1]);
    const unsigned u2 = pk2(bflo(u1) * 0.08838834764831845f, bfhi(u1) * 0.08838834764831845f);
    sc[k] = bflo(u2); sc[k + 1] = bfhi(u2);
    mx = fmaxf(mx, fmaxf(sc[k], sc[k + 1]));
  }
  mx = fmaxf(mx, dpp<XOR1>(mx)); mx = fmaxf(mx, dpp<XOR2>(mx));
  mx = x32_max(x16_max(mx));
  if (lane < 16 && jj == 0) *(LAS float*)(L + L_MX + hd * 16 + kw * 4) = mx;
  DRAIN_LGKM();
  lds_add1(ct + C_MAX, lane);
  lds_wait_ge(ct + C_MAX, 4u);
  const f32x4 m4 = *(LAS f32x4*)(L + L_MX + hd * 16);
  const float m2 = fmaxf(fmaxf(m4.x, m4.y), fmaxf(m4.z, m4.w)) * 1.4426950408889634f;
  float ls = 0.f;
  {
    v4u P[2];
#pragma unroll
    for (int k = 0; k < 8; ++k) { const float p = __builtin_amdgcn_exp2f(fmaf(sc[k], 1.4426950408889634f, -m2)); ls += p; P[k >> 2][k & 3] = pk2(p, p); }
    *(LAS v4u*)(kb + lane * 16) = P[0]; *(LAS v4u*)(kb + 1024 + lane * 16) = P[1];
  }
  ls += dpp<XOR1>(ls); ls += dpp<XOR2>(ls);
  ls = x32_sum(x16_sum(ls));
  if (lane < 16 && jj == 0) *(LAS float*)(L + L_LX + hd * 16 + kw * 4) = ls;
  DRAIN_LGKM();
  if (lane == 0) lds_st(ct + C_PRDY + kw, 1u);
  lds_add1(ct + C_PCNT, lane);
  if (kw == 0) {
    STAMP(6);
    lds_wait_ge(ct + C_PCNT, 4u);
    const f32x4 l4 = *(LAS f32x4*)(L + L_LX + hd * 16);
    const float lt = (l4.x + l4.y) + (l4.z + l4.w);
    if (lane < 16 && jj == 0) {
      const v2u ml = {min(__float_as_uint(m2), SENT - 1), min(__float_as_uint(lt), SENT - 1)};
      __builtin_amdgcn_raw_buffer_store_b64(ml, rw, WS_ML + ((hi2 * 32 + 4 * grp + hd) * 8 + lo3) * 8, 0, AUX_SC1);
    }
  }
  if (kw == 1) {
#pragma unroll 1
    for (unsigned n = 0; n < GSPIN; ++n) {
      const v4u a = __builtin_amdgcn_raw_buffer_load_b128(rw, WS_FLAG + lane * 16, 0, AUX_SC1), d = __builtin_amdgcn_raw_buffer_load_b128(rw, WS_FLAG + 1024 + lane * 16, 0, AUX_SC1);
      const unsigned bad = (a.x ^ tag.x) | (a.y ^ tag.y) | (a.z ^ tag.x) | (a.w ^ tag.y) | (d.x ^ tag.x) | (d.y ^ tag.y) | (d.z ^ tag.x) | (d.w ^ tag.y);
      if (__all(bad == 0)) break;
      MEMFENCE();
      __builtin_amdgcn_s_sleep(8);
    }
    MEMFENCE();
    if (lane == 0) lds_st(ct + C_CERT, 1u);
    v4u g[8];
    const int j = min(c, 13);
#pragma unroll
    for (int k = 0; k < 8; ++k) g[k] = __builtin_amdgcn_raw_buffer_load_b128(rw, WS_GP + ((k * 4 + r) * 1792 + 56 * hh) * 4 + j * 16, 0, AUX_SC1);
    const f32x4 sg = (__builtin_bit_cast(f32x4, g[0]) + __builtin_bit_cast(f32x4, g[1])) + (__builtin_bit_cast(f32x4, g[2]) + __builtin_bit_cast(f32x4, g[3]));
    const f32x4 su = (__builtin_bit_cast(f32x4, g[4]) + __builtin_bit_cast(f32x4, g[5])) + (__builtin_bit_cast(f32x4, g[6]) + __builtin_bit_cast(f32x4, g[7]));
    v2u hv = {pk2(rbf(sg.x) * rbf(su.x), rbf(sg.y) * rbf(su.y)), pk2(rbf(sg.z) * rbf(su.z), rbf(sg.w) * rbf(su.w))};
    if (c >= 14) hv = v2u{0u, 0u};
    *(LAS v2u*)(L + L_HS + r * 192 + c * 8) = hv;
    STAMP(7);
  } else if (kw >= 2) {
    lds_wait_ge(ct + C_VISS, 4u);
    const __amdgpu_buffer_rsrc_t ro = __builtin_amdgcn_make_buffer_rsrc((void*)wo, 0, 4194304, RSRC_W3);
    const __amdgpu_buffer_rsrc_t rd = __builtin_amdgcn_make_buffer_rsrc((void*)wd, 0, 1792 * 512 * 2, RSRC_W3);
    const int r8 = lane >> 3, c8 = lane & 7, hf = kw - 2;
#pragma unroll
    for (int ii = 0; ii < 8; ++ii) {
      const int dl = 8 * (r8 & 3) + (r8 >> 2), du = 32 * ((ii >> 1) & 3) + 4 * hf + 2 * (ii & 1);
      const int f = ((r8 >> 1) & 1) | ((ii & 1) << 1);
      __builtin_amdgcn_raw_ptr_buffer_load_lds(ro, (LAS void*)(L + L_WO + (8 * hf + ii) * 1024), 16, dl * 1024 + ((c8 ^ (2 * f)) * 16), (hh * 128 + du) * 1024 + lo3 * 128, 0, AUX_NT);
    }
#pragma unroll
    for (int ii = 0; ii < 4; ++ii) {
      const int i = 4 * hf + ii;
      if (i < 7) {
        const int pos = 56 * hh + 8 * i + r8, row = (pos & ~63) + pos2col(pos & 63);
        const int f = ((r8 >> 1) & 1) | ((ii & 1) << 1);
        __builtin_amdgcn_raw_ptr_buffer_load_lds(rd, (LAS void*)(L + L_WD + i * 1024), 16, row * 1024 + ((c8 ^ (2 * f)) * 16), lo3 * 128, 0, AUX_NT);
      }
    }
  }
  SB();
  {
    v2u pc;
#pragma unroll 1
    for (unsigned n = 0; n < LSPIN; ++n) {
      asm volatile("ds_read_b32 %0, %2\n\tds_read_b32 %1, %2 offset:28\n\ts_waitcnt lgkmcnt(0)" : "=&v"(pc.x), "=&v"(pc.y) : "v"((unsigned)(size_t)(ct + C_PUB)) : "memory");
      if (__builtin_amdgcn_readfirstlane(pc.x & pc.y) != 0) break;
      __builtin_amdgcn_s_sleep(1);
    }
  }
  if (kw == 0) STAMP(11);
  {
    const int sp = lane >> 5;
    v4u o[4], a, d;
    const int off = WS_O + ((kw * 32 + hh) * 8 + 4 * sp) * 512 + (lane & 31) * 16, offm = WS_ML + ((kw * 32 + hh) * 8 + 4 * sp) * 8;
#pragma unroll 1
    for (unsigned n = 0; n < GSPIN; ++n) {
      a = __builtin_amdgcn_raw_buffer_load_b128(rw, offm, 0, AUX_SC1);
      d = __builtin_amdgcn_raw_buffer_load_b128(rw, offm + 16, 0, AUX_SC1);
#pragma unroll
      for (int k = 0; k < 4; ++k) o[k] = __builtin_amdgcn_raw_buffer_load_b128(rw, off + k * 512, 0, AUX_SC1);
      if (__all(max(max(max(umax4(o[0]), umax4(o[1])), max(umax4(o[2]), umax4(o[3]))), max(umax4(a), umax4(d))) != SENT)) break;
      MEMFENCE();
    }
    if (kw == 0) STAMP(12);
    const float m0 = __uint_as_float(a.x), m1 = __uint_as_float(a.z), m2_ = __uint_as_float(d.x), m3 = __uint_as_float(d.z);
    const float M = x32_max(fmaxf(fmaxf(m0, m1), fmaxf(m2_, m3)));
    const float w0 = __builtin_amdgcn_exp2f(m0 - M), w1 = __builtin_amdgcn_exp2f(m1 - M), w2 = __builtin_amdgcn_exp2f(m2_ - M), w3 = __builtin_amdgcn_exp2f(m3 - M);
    const float iL = __builtin_amdgcn_rcpf(x32_sum((w0 * __uint_as_float(a.y) + w1 * __uint_as_float(a.w)) + (w2 * __uint_as_float(d.y) + w3 * __uint_as_float(d.w))));
    float y[4];
#pragma unroll
    for (int t = 0; t < 4; ++t) y[t] = (w0 * __uint_as_float(o[0][t]) + w1 * __uint_as_float(o[1][t])) + (w2 * __uint_as_float(o[2][t]) + w3 * __uint_as_float(o[3][t]));
    auto e0 = __builtin_amdgcn_permlane32_swap(__float_as_uint(y[0]), __float_as_uint(y[2]), false, false);
    auto e1 = __builtin_amdgcn_permlane32_swap(__float_as_uint(y[1]), __float_as_uint(y[3]), false, false);
    *(LAS unsigned*)(L + L_OS + kw * 320 + (lane & 31) * 8 + sp * 4) =
        pk2((__uint_as_float(e0[0]) + __uint_as_float(e0[1])) * iL, (__uint_as_float(e1[0]) + __uint_as_float(e1[1])) * iL);
  }
  asm volatile("s_waitcnt vmcnt(0) lgkmcnt(0)" ::: "memory");
  __builtin_amdgcn_s_barrier();
  MEMFENCE();
  {
    const int ta = (8 * r + hd) * 128 + ((kw ^ ((hd >> 1) | ((r & 1) << 1))) * 32) + jj * 8;
    v4u af[6], bf[6];
#pragma unroll
    for (int ks = 0; ks < 4; ++ks) {
      af[ks] = *(LAS v4u*)(L + L_OS + jj * 320 + ks * 64 + r * 16);
      const v2u t0 = trrd(L + L_WO + ta + ks * 4096), t1 = trrd(L + L_WO + ta + ks * 4096 + 512);
      bf[ks] = v4u{t0.x, t0.y, t1.x, t1.y};
    }
#pragma unroll
    for (int ks = 0; ks < 2; ++ks) {
      af[4 + ks] = *(LAS v4u*)(L + L_HS + jj * 192 + ks * 64 + r * 16);
      const v2u t0 = trrd(L + L_WD + ta + ks * 4096), t1 = trrd(L + L_WD + ta + ks * 4096 + 512);
      bf[4 + ks] = v4u{t0.x, t0.y, t1.x, t1.y};
    }
    SB();
    f32x4 acc = {0, 0, 0, 0};
#pragma unroll
    for (int ks = 0; ks < 6; ++ks) acc = mfma32(af[ks], bf[ks], acc);
    if (lane < 16) __builtin_amdgcn_raw_buffer_store_b128(canon(acc), rw, WS_P2 + (lo3 * 32 + hh) * 1024 + (16 * kw + lane) * 16, 0, AUX_SC1);
  }
  if (kw == 0) STAMP(13);
}

extern "C" void kernel_launch(void* const* d_in, const int* in_sizes, int n_in, void* d_out, int out_size, void* d_ws, size_t ws_size, hipStream_t stream) {
  fused_decode<<<NWG, NTHR, 0, stream>>>((const char*)d_in[0], (const char*)d_in[1], (const char*)d_in[2], (const char*)d_in[3], (const char*)d_in[4],
                                         (const char*)d_in[6], (const char*)d_in[7], (const char*)d_in[8], (unsigned short*)d_out, (char*)d_ws);
}
```

```cpp
#include <hip/hip_runtime.h>
#include <hip/hip_bf16.h>
#include <stdint.h>

#define LAS __attribute__((address_space(3)))
#define GAS __attribute__((address_space(1)))
typedef unsigned v4u __attribute__((ext_vector_type(4)));
typedef unsigned v2u __attribute__((ext_vector_type(2)));
typedef float f32x4 __attribute__((ext_vector_type(4)));
typedef short s16x4 __attribute__((ext_vector_type(4)));
typedef __bf16 bf16x8 __attribute__((ext_vector_type(8)));
typedef __bf16 bf16x2 __attribute__((ext_vector_type(2)));
typedef float f32x2 __attribute__((ext_vector_type(2)));
typedef unsigned long long u64;

#define NFL 12
#define QTRIG 16
#define TAU16 224
#define POFF 6
#define NSIG 8
#define PV0 20
#define MLTRIG 22
#define PDELAY 6
#define OUT_AUX 0
#define STAMP_BLOCKS 256
#define NSTAMP 16

constexpr int NWG = 256, NTHR = 512;
constexpr int AUX_NT = 2, AUX_SC1 = 16, RSRC_W3 = 0x00020000;
constexpr int WS_EPOCH = 0, WS_FLAG = 2048, WS_ML = 4096, WS_QP = WS_ML + 4 * 32 * 64, WS_GP = WS_QP + 4 * 4 * 4096 * 8,
              WS_O = WS_GP + 2 * 4 * 4 * 1792 * 4, WS_P2 = WS_O + 4 * 32 * 8 * 512, WS_END = WS_P2 + 8 * 32 * 1024;
constexpr int L_K = 0, L_WO = 131072, L_WD = L_WO + 16384, L_QS = L_WD + 8192, L_OS = L_QS + 1280, L_HS = L_OS + 1280, L_XS = L_HS + 768,
              L_MX = L_XS + 2176, L_LX = L_MX + 64, L_CT = L_LX + 64, L_END = L_CT + 128;
enum { C_QRDY = 0, C_MAX = 1, C_PRDY = 2  , C_PCNT = 6, C_MRG = 7, C_PUB = 8, C_VISS = 9, C_KRDY = 10  , C_QTRIG = 14, C_CERT = 15, C_MLT = 16, C_WRDY = 17 };
constexpr unsigned SENT = 0xFFFFFFFFu;
constexpr unsigned GSPIN = 1u << 16, LSPIN = 1u << 19;

__device__ __forceinline__ float bflo(unsigned u) { return __builtin_bit_cast(float, u << 16); }
__device__ __forceinline__ float bfhi(unsigned u) { return __builtin_bit_cast(float, u & 0xffff0000u); }
__device__ __forceinline__ unsigned pk2(float lo, float hi) { f32x2 f = {lo, hi}; return __builtin_bit_cast(unsigned, __builtin_convertvector(f, bf16x2)); }
__device__ __forceinline__ float rbf(float f) { return bflo(pk2(f, f)); }
template <int CTRL> __device__ __forceinline__ float dpp(float x) { return __builtin_bit_cast(float, __builtin_amdgcn_mov_dpp(__builtin_bit_cast(int, x), CTRL, 0xf, 0xf, true)); }
template <int CTRL> __device__ __forceinline__ unsigned dppu(unsigned x) { return (unsigned)__builtin_amdgcn_mov_dpp((int)x, CTRL, 0xf, 0xf, true); }
constexpr int XOR1 = 0xB1, XOR2 = 0x4E, HMIR = 0x141, ROR8 = 0x128, ROR4 = 0x124;
__device__ __forceinline__ float row16_sum(float x) { x += dpp<XOR1>(x); x += dpp<XOR2>(x); x += dpp<HMIR>(x); x += dpp<ROR8>(x); return x; }
__device__ __forceinline__ float x16_sum(float x) { auto s = __builtin_amdgcn_permlane16_swap(__float_as_uint(x), __float_as_uint(x), false, false); return __uint_as_float(s[0]) + __uint_as_float(s[1]); }
__device__ __forceinline__ float x32_sum(float x) { auto s = __builtin_amdgcn_permlane32_swap(__float_as_uint(x), __float_as_uint(x), false, false); return __uint_as_float(s[0]) + __uint_as_float(s[1]); }
__device__ __forceinline__ float x16_max(float x) { auto s = __builtin_amdgcn_permlane16_swap(__float_as_uint(x), __float_as_uint(x), false, false); return fmaxf(__uint_as_float(s[0]), __uint_as_float(s[1])); }
__device__ __forceinline__ float x32_max(float x) { auto s = __builtin_amdgcn_permlane32_swap(__float_as_uint(x), __float_as_uint(x), false, false); return fmaxf(__uint_as_float(s[0]), __uint_as_float(s[1])); }
__device__ __forceinline__ f32x4 mfma16(v2u a, v2u b, f32x4 c) { return __builtin_amdgcn_mfma_f32_16x16x16bf16_1k(__builtin_bit_cast(s16x4, a), __builtin_bit_cast(s16x4, b), c, 0, 0, 0); }
__device__ __forceinline__ f32x4 mfma32(v4u a, v4u b, f32x4 c) { return __builtin_amdgcn_mfma_f32_16x16x32_bf16(__builtin_bit_cast(bf16x8, a), __builtin_bit_cast(bf16x8, b), c, 0, 0, 0); }
__device__ __forceinline__ unsigned umax3(unsigned a, unsigned b, unsigned c) { return max(max(a, b), c); }
__device__ __forceinline__ unsigned umax4(v4u u) { return max(umax3(u.x, u.y, u.z), u.w); }
__device__ __forceinline__ v2u trrd(LAS char* p) { return __builtin_bit_cast(v2u, __builtin_amdgcn_ds_read_tr16_b64_v4i16((LAS s16x4*)p)); }
#define DRAIN_VM() asm volatile("s_waitcnt vmcnt(0)" ::: "memory")
#define DRAIN_LGKM() asm volatile("s_waitcnt lgkmcnt(0)" ::: "memory")
#define MEMFENCE() asm volatile("" ::: "memory")
#define SB() __builtin_amdgcn_sched_barrier(0)
#define KEEP(x) asm volatile("" : "+v"(x))

__device__ __forceinline__ void lds_wait_ge(LAS unsigned* p, unsigned want) {
  const unsigned a = (unsigned)(size_t)p;
#pragma unroll 1
  for (unsigned n = 0; n < LSPIN; ++n) {
    unsigned v;
    asm volatile("ds_read_b32 %0, %1\n\ts_waitcnt lgkmcnt(0)" : "=v"(v) : "v"(a) : "memory");
    if ((unsigned)__builtin_amdgcn_readfirstlane(v) >= want) break;
    __builtin_amdgcn_s_sleep(1);
  }
}
__device__ __forceinline__ void lds_set1(LAS unsigned* p) { asm volatile("ds_write_b32 %0, %1" :: "v"((unsigned)(size_t)p), "v"(1u) : "memory"); }
__device__ __forceinline__ void lds_add1(LAS unsigned* p, int lane) { if (lane == 0) asm volatile("ds_add_u32 %0, %1" :: "v"((unsigned)(size_t)p), "v"(1u) : "memory"); }
#define STAMP(k) do {} while (0)
#define LDS_EXTRA 0

__device__ __forceinline__ int pos2col(int pos) { return 32 * ((pos >> 4) & 1) + 8 * (pos & 3) + 4 * (pos >> 5) + ((pos >> 2) & 3); }

__device__ __forceinline__ f32x4 ln_tile(const v4u (&xr)[4], const v4u (&w)[16], LAS char* xs, int lane) {
  float e[32];
#pragma unroll
  for (int k = 0; k < 4; ++k)
#pragma unroll
    for (int j = 0; j < 4; ++j) { e[8 * k + 2 * j] = bflo(xr[k][j]); e[8 * k + 2 * j + 1] = bfhi(xr[k][j]); }
  float s0 = 0.f, s1 = 0.f;
#pragma unroll
  for (int k = 0; k < 16; ++k) { s0 += e[2 * k]; s1 += e[2 * k + 1]; }
  const float mean = row16_sum(s0 + s1) * (1.f / 512.f);
  float q0 = 0.f, q1 = 0.f;
#pragma unroll
  for (int k = 0; k < 16; ++k) { float d0 = e[2 * k] - mean, d1 = e[2 * k + 1] - mean; q0 = fmaf(d0, d0, q0); q1 = fmaf(d1, d1, q1); }
  const float var = rbf(row16_sum(q0 + q1) * (1.f / 512.f));
  const float mb = rbf(mean), den = rbf(sqrtf(rbf(var + 1e-5f))), inv = 1.f / den;
  v4u xo;
#pragma unroll
  for (int j = 0; j < 4; ++j) xo[j] = pk2(rbf(e[2 * j] - mb) * inv, rbf(e[2 * j + 1] - mb) * inv);
  *(LAS v4u*)(xs + (lane >> 4) * 272 + (lane & 15) * 16) = xo;
  DRAIN_LGKM();
  const int g = lane >> 4, bb = (lane >> 2) & 3, jj = lane & 3;
  unsigned pr[16];
#pragma unroll
  for (int i = 0; i < 16; ++i) pr[i] = *(LAS unsigned*)(xs + bb * 272 + (8 * i + 2 * g) * 2);
  SB();
  const unsigned z = 0x0c0c0c0cu;
  const unsigned sA0 = jj == 0 ? 0x0c0c0100u : jj == 1 ? 0x01000c0cu : z, sB0 = jj == 2 ? 0x0c0c0100u : jj == 3 ? 0x01000c0cu : z;
  const unsigned sA1 = jj == 0 ? 0x0c0c0302u : jj == 1 ? 0x03020c0cu : z, sB1 = jj == 2 ? 0x0c0c0302u : jj == 3 ? 0x03020c0cu : z;
  f32x4 d00 = {0, 0, 0, 0}, d01 = d00, d10 = d00, d11 = d00;
#pragma unroll
  for (int i = 0; i < 16; ++i) {
    const v2u b0 = {__builtin_amdgcn_perm(pr[i], pr[i], sA0), __builtin_amdgcn_perm(pr[i], pr[i], sB0)};
    const v2u b1 = {__builtin_amdgcn_perm(pr[i], pr[i], sA1), __builtin_amdgcn_perm(pr[i], pr[i], sB1)};
    const v2u lo = {w[i].x, w[i].y}, hi = {w[i].z, w[i].w};
    d00 = mfma16(lo, b0, d00); d01 = mfma16(hi, b0, d01); d10 = mfma16(lo, b1, d10); d11 = mfma16(hi, b1, d11);
  }
  f32x4 y;
#pragma unroll
  for (int t = 0; t < 4; ++t) {
    auto r = __builtin_amdgcn_permlane32_swap(__float_as_uint(d10[t]), __float_as_uint(d01[t]), false, false);
    y[t] = lane < 32 ? d00[t] + __uint_as_float(r[1]) : d11[t] + __uint_as_float(r[0]);
  }
  return y;
}

constexpr int pv_done(int i) { return i < PV0 ? 0 : (3 * (i - PV0 + 1) < i - 6 ? 3 * (i - PV0 + 1) : i - 6); }

template <int J> __device__ __forceinline__ void pv_step(const v4u& v, const v4u& P0, const v4u& P1, unsigned mk0, unsigned mk1, f32x4& a0, f32x4& a1) {
  const unsigned s = (J < 16 ? P0 : P1)[J & 3];
  const v2u p = {dppu<0x55 * ((J >> 2) & 3)>(s) & mk0, dppu<0x55 * ((J >> 2) & 3)>(s) & mk1};
  a0 = mfma16(v2u{v.x, v.y}, p, a0); a1 = mfma16(v2u{v.z, v.w}, p, a1);
}
template <int LO, int HI> __device__ __forceinline__ void pv_range(const v4u (&v)[32], const v4u& P0, const v4u& P1, unsigned mk0, unsigned mk1, f32x4& a0, f32x4& a1) {
  if constexpr (LO < HI) { pv_step<LO>(v[LO], P0, P1, mk0, mk1, a0, a1); pv_range<LO + 1, HI>(v, P0, P1, mk0, mk1, a0, a1); }
}

__global__ void __launch_bounds__(NTHR, 2)
fused_decode(const char* __restrict__ x, const char* __restrict__ Kc, const char* __restrict__ Vc, const char* __restrict__ wq, const char* __restrict__ wo,
             const char* __restrict__ wg, const char* __restrict__ wu, const char* __restrict__ wd, char* __restrict__ out, char* ws) {
  __shared__ __attribute__((aligned(16))) char smem_[L_END + LDS_EXTRA];
  LAS char* L = (LAS char*)smem_;
  LAS unsigned* ct = (LAS unsigned*)(L + L_CT);
  const unsigned tb = (unsigned)__builtin_amdgcn_s_memrealtime();
  const int lane = threadIdx.x & 63, wave = __builtin_amdgcn_readfirstlane(threadIdx.x >> 6), blk = blockIdx.x;
  const int grp = blk & 7, mem = blk >> 3, hi2 = mem >> 3, lo3 = mem & 7;
  const int hh = 4 * grp + hi2;
  asm volatile("" :: "s"(x), "s"(Kc), "s"(Vc), "s"(wq), "s"(wo), "s"(wg), "s"(wu), "s"(wd), "s"(out), "s"(ws));
  const __amdgpu_buffer_rsrc_t rw = __builtin_amdgcn_make_buffer_rsrc((void*)ws, 0, WS_END, RSRC_W3);
  const int r = lane >> 4, c = lane & 15, hd = (lane >> 2) & 3, jj = lane & 3;

  if (wave >= 4) {
    const int sw = wave - 4, pair = hi2 * 8 + grp;
    if (sw == 0) STAMP(0);
    const __amdgpu_buffer_rsrc_t rk = __builtin_amdgcn_make_buffer_rsrc((void*)(Kc + (size_t)pair * 1048576), 0, 1048576, RSRC_W3);
    const __amdgpu_buffer_rsrc_t rv = __builtin_amdgcn_make_buffer_rsrc((void*)(Vc + (size_t)pair * 1048576), 0, 1048576, RSRC_W3);
    LAS char* kb = L + L_K + sw * 32768;
#define KVOFF(i) (((((i) + pair) & 31) << 15) + (((lo3 + (i)) & 7) << 12) + (sw << 10))
#define PACE(k) do { const unsigned tg_ = tb + ((((k) + POFF) * TAU16) >> 4); while ((int)((unsigned)__builtin_amdgcn_s_memrealtime() - tg_) < 0) {} } while (0)
#define VMWAIT(n) asm volatile("s_waitcnt vmcnt(%0)" :: "i"(n) : "memory")
    __builtin_amdgcn_s_barrier();
    SB();
    const int col0 = grp * 64 + mem * 2;
    const unsigned resid_raw = __builtin_amdgcn_raw_buffer_load_b16(__builtin_amdgcn_make_buffer_rsrc((void*)x, 0, 4096, RSRC_W3), (r * 512 + col0 + (lane & 1)) * 2, 0, 0);
    const v2u ep = __builtin_amdgcn_raw_buffer_load_b64(rw, WS_EPOCH, 0, AUX_SC1);
    SB();
#pragma unroll
    for (int i = 0; i < 32; ++i) {
      PACE(i);
      __builtin_amdgcn_raw_ptr_buffer_load_lds(rk, (LAS void*)(kb + i * 1024), 16, r * 256 + ((c ^ (4 * r + (i & 3))) * 16), KVOFF(i), 0, AUX_NT);
      if (i >= NFL - 1) VMWAIT(NFL - 1);
      if (i == QTRIG + NSIG - 2) { VMWAIT(NSIG - 1); lds_set1(ct + C_QTRIG); }
      SB();
    }
    v4u v[32], P0, P1;
    const unsigned mk0 = jj == 0 ? 0x0000ffffu : jj == 1 ? 0xffff0000u : 0u, mk1 = jj == 2 ? 0x0000ffffu : jj == 3 ? 0xffff0000u : 0u;
    f32x4 a0 = {0, 0, 0, 0}, a1 = a0;
    int offo = WS_O + ((hi2 * 32 + 4 * grp + hd) * 8 + lo3) * 512 + r * 64 + jj * 16;
    KEEP(offo);
#define VSTEP(i) do { \
      PACE(32 + (i)); \
      v[i] = __builtin_amdgcn_raw_buffer_load_b128(rv, lane * 16, KVOFF(i), AUX_NT); \
      if ((i) < PV0) VMWAIT(NFL - 1); \
      if ((i) == NSIG - 2) { VMWAIT(NSIG - 1); lds_set1(ct + C_KRDY + sw); if (sw == 0) STAMP(5); } \
      if ((i) == MLTRIG) lds_set1(ct + C_MLT); \
      if ((i) == 31) { lds_add1(ct + C_VISS, lane); __builtin_amdgcn_s_setprio(3); if (sw == 3) STAMP(8); } \
      SB(); \
      if ((i) == PV0) { lds_wait_ge(ct + C_PRDY + sw, 1u); P0 = *(LAS v4u*)(kb + lane * 16); P1 = *(LAS v4u*)(kb + 1024 + lane * 16); } \
      pv_range<pv_done((i) - 1), pv_done(i)>(v, P0, P1, mk0, mk1, a0, a1); \
      SB(); } while (0)
#define VSTEP4(i) VSTEP(i); VSTEP(i + 1); VSTEP(i + 2); VSTEP(i + 3)
    VSTEP4(0); VSTEP4(4); VSTEP4(8); VSTEP4(12); VSTEP4(16); VSTEP4(20); VSTEP4(24); VSTEP4(28);
    pv_range<pv_done(31), 32>(v, P0, P1, mk0, mk1, a0, a1);
    *(LAS f32x4*)(kb + 16384 + lane * 16) = a0; *(LAS f32x4*)(kb + 17408 + lane * 16) = a1;
    unsigned old;
    asm volatile("s_waitcnt lgkmcnt(0)\n\tds_add_rtn_u32 %0, %1, %2\n\ts_waitcnt lgkmcnt(0)" : "=&v"(old) : "v"((unsigned)(size_t)(ct + C_MRG)), "v"(lane == 0 ? 1u : 0u) : "memory");
    old = __builtin_amdgcn_readfirstlane(old);
    if (old == 3u) {
      STAMP(9);
      LAS char* mb = L + L_K + 16384 + lane * 16;
      f32x4 t0[4], t1[4];
#pragma unroll
      for (int w = 0; w < 4; ++w) t0[w] = *(LAS f32x4*)(mb + w * 32768);
#pragma unroll
      for (int w = 0; w < 4; ++w) t1[w] = *(LAS f32x4*)(mb + w * 32768 + 1024);
      SB();
      __builtin_amdgcn_raw_buffer_store_b128(__builtin_bit_cast(v4u, (t0[0] + t0[1]) + (t0[2] + t0[3])), rw, offo, 0, AUX_SC1);
      __builtin_amdgcn_raw_buffer_store_b128(__builtin_bit_cast(v4u, (t1[0] + t1[1]) + (t1[2] + t1[3])), rw, offo + 256, 0, AUX_SC1);
      lds_set1(ct + C_PUB);
      STAMP(10);
    }
    if (sw != 0) return;
    float resid = bflo(resid_raw);
    const u64 tg = (((u64)ep.y << 32) | ep.x) + 1ull;
    v2u tag = v2u{(unsigned)tg, (unsigned)(tg >> 32)};
    int off = WS_P2 + (grp * 32 + (lane >> 1)) * 1024 + (mem * 2 + (lane & 1)) * 16;
    int oo = c < 2 ? (r * 512 + col0 + c) * 2 : 0x7fffffff;
    int oe = (blk == 0 && lane == 0) ? WS_EPOCH : 0x7fffffff;
    const __amdgpu_buffer_rsrc_t rout = __builtin_amdgcn_make_buffer_rsrc((void*)out, 0, 4096, RSRC_W3);
    KEEP(resid); KEEP(tag); KEEP(off); KEEP(oo); KEEP(oe);
    __builtin_amdgcn_s_barrier();
    MEMFENCE();
    v4u pp;
#pragma unroll 1
    for (unsigned n = 0; n < GSPIN; ++n) {
      pp = __builtin_amdgcn_raw_buffer_load_b128(rw, off, 0, AUX_SC1);
      if (__all(umax4(pp) != SENT)) break;
      MEMFENCE();
    }
    STAMP(14);
    {
      auto u0 = __builtin_amdgcn_permlane32_swap(pp.x, pp.z, false, false);
      auto u1 = __builtin_amdgcn_permlane32_swap(pp.y, pp.w, false, false);
      const float e0 = __uint_as_float(u0[0]) + __uint_as_float(u0[1]), e1 = __uint_as_float(u1[0]) + __uint_as_float(u1[1]);
      auto u2 = __builtin_amdgcn_permlane16_swap(__float_as_uint(e0), __float_as_uint(e1), false, false);
      float f = __uint_as_float(u2[0]) + __uint_as_float(u2[1]);
      f += dpp<XOR2>(f); f += dpp<ROR4>(f); f += dpp<ROR8>(f);
      f += resid;
      __builtin_amdgcn_raw_buffer_store_b16((unsigned short)pk2(f, f), rout, oo, 0, OUT_AUX);
      __builtin_amdgcn_raw_buffer_store_b64(tag, rw, oe, 0, AUX_SC1);
    }
    STAMP(15);
    return;
  }
  const int kw = wave;
  __builtin_amdgcn_s_setprio(2);
  v2u tag = {0u, 0u};
  if (kw < 2) {
    const __amdgpu_buffer_rsrc_t rx = __builtin_amdgcn_make_buffer_rsrc((void*)x, 0, 4096, RSRC_W3);
    LAS char* xs = L + L_XS + kw * 1088;
    const int which = blk >= 112, tt = blk - 112 * which, rsg = tt / 28, cgg = tt - 28 * rsg;
    const bool isq = kw == 0, have = isq || blk < 224;
    const int rs = isq ? hi2 : rsg, pitch = isq ? 8192 : 3584, cb = isq ? (grp * 512 + lo3 * 64) * 2 : cgg * 128;
    const __amdgpu_buffer_rsrc_t rq = __builtin_amdgcn_make_buffer_rsrc((void*)(isq ? wq : which ? wu : wg), 0, have ? 512 * pitch : 0, RSRC_W3);
    v4u xr[4], w[16];
#pragma unroll
    for (int k = 0; k < 4; ++k) xr[k] = __builtin_amdgcn_raw_buffer_load_b128(rx, r * 1024 + c * 16, ((rs + k) & 3) * 256, 0);
#pragma unroll
    for (int i = 0; i < 16; ++i) w[i] = __builtin_amdgcn_raw_buffer_load_b128(rq, (lane >> 3) * pitch + (lane & 7) * 16, (rs * 128 + 8 * i) * pitch + cb, AUX_NT);
    v2u ep = __builtin_amdgcn_raw_buffer_load_b64(rw, WS_EPOCH, 0, AUX_SC1);
    if (!isq) {
      const v4u sent = {SENT, SENT, SENT, SENT};
      const int offo = WS_O + ((hi2 * 32 + 4 * grp + hd) * 8 + lo3) * 512 + r * 64 + jj * 16;
      __builtin_amdgcn_raw_buffer_store_b128(sent, rw, offo, 0, AUX_SC1);
      __builtin_amdgcn_raw_buffer_store_b128(sent, rw, offo + 256, 0, AUX_SC1);
      __builtin_amdgcn_raw_buffer_store_b128(sent, rw, WS_P2 + (lo3 * 32 + hh) * 1024 + lane * 16, 0, AUX_SC1);
      if (lane < 4) __builtin_amdgcn_raw_buffer_store_b64(v2u{SENT, SENT}, rw, WS_ML + ((hi2 * 32 + 4 * grp + lane) * 8 + lo3) * 8, 0, AUX_SC1);
    }
    SB();
    __builtin_amdgcn_s_barrier();
    SB();
    const f32x4 y = ln_tile(xr, w, xs, lane);
    SB();
    KEEP(ep);
    const u64 tg = (((u64)ep.y << 32) | ep.x) + 1ull;
    tag = v2u{(unsigned)tg, (unsigned)(tg >> 32)};
    if (isq) {
      STAMP(1);
      const unsigned t8 = tag.y & 0xffu;
      const int po = WS_QP + ((hi2 * 4 + hd) * 4096 + grp * 512 + lo3 * 64) * 8 + (r * 4 + jj) * 16;
      const v4u yu = __builtin_bit_cast(v4u, y);
      __builtin_amdgcn_raw_buffer_store_b128(v4u{(yu.x & ~0xffu) | t8, tag.x, (yu.y & ~0xffu) | t8, tag.x}, rw, po, 0, AUX_SC1);
      __builtin_amdgcn_raw_buffer_store_b128(v4u{(yu.z & ~0xffu) | t8, tag.x, (yu.w & ~0xffu) | t8, tag.x}, rw, po + 256, 0, AUX_SC1);
      lds_wait_ge(ct + C_QTRIG, 1u);
      STAMP(3);
      v4u g[16];
#pragma unroll 1
      for (unsigned n = 0; n < GSPIN; ++n) {
        unsigned bad = 0;
#pragma unroll
        for (int k = 0; k < 16; ++k) g[k] = __builtin_amdgcn_raw_buffer_load_b128(rw, WS_QP + lane * 16, (((k & 3) * 4 + hi2) * 4096 + grp * 512) * 8 + (k >> 2) * 1024, AUX_SC1);
#pragma unroll
        for (int k = 0; k < 16; ++k) bad |= (g[k].y ^ tag.x) | (g[k].w ^ tag.x) | (((g[k].x ^ t8) | (g[k].z ^ t8)) & 0xffu);
        if (__all(bad == 0)) break;
        MEMFENCE();
        __builtin_amdgcn_s_sleep(8);
      }
#pragma unroll
      for (int u = 0; u < 4; ++u) {
        float s0 = 0.f, s1 = 0.f;
#pragma unroll
        for (int k = 0; k < 4; ++k) { s0 += __uint_as_float(g[4 * u + k].x & ~0xffu); s1 += __uint_as_float(g[4 * u + k].z & ~0xffu); }
        const int col = (2 * u + (r >> 1)) * 64 + pos2col(4 * c) + 16 * (r & 1);
        LAS char* q = L + L_QS + (col >> 7) * 320 + (col & 127) * 2;
        const unsigned a = pk2(s0, s1);
        *(LAS unsigned short*)(q) = (unsigned short)a; *(LAS unsigned short*)(q + 16) = (unsigned short)(a >> 16);
      }
      DRAIN_LGKM();
      lds_set1(ct + C_QRDY);
      STAMP(4);
    } else {
      if (have) __builtin_amdgcn_raw_buffer_store_b128(__builtin_bit_cast(v4u, y), rw, WS_GP + (((which * 4 + rsg) * 4 + hd) * 1792 + cgg * 64) * 4 + (r * 4 + jj) * 16, 0, AUX_SC1);
      DRAIN_VM();
      if (lane == 0) __builtin_amdgcn_raw_buffer_store_b64(tag, rw, WS_FLAG + blk * 8, 0, AUX_SC1);
    }
  } else {
    if (kw == 2) { if (lane < 32) ct[lane] = 0u; *(LAS v4u*)(L + L_WD + 7168 + lane * 16) = v4u{0, 0, 0, 0}; DRAIN_LGKM(); }
    __builtin_amdgcn_s_barrier();
  }
  LAS char* kb = L + L_K + kw * 32768;
  lds_wait_ge(ct + C_QRDY, 1u);
  lds_wait_ge(ct + C_KRDY + kw, 1u);
  float sc[8];
  {
    v4u qf[4];
#pragma unroll
    for (int ks = 0; ks < 4; ++ks) qf[ks] = *(LAS v4u*)(L + L_QS + hd * 320 + ks * 64 + r * 16);
    f32x4 S[8];
    LAS char* ka = kb + (4 * (c & 3) + (c >> 2)) * 256;
#pragma unroll
    for (int T = 0; T < 8; ++T) {
      f32x4 a = {0, 0, 0, 0};
#pragma unroll
      for (int ks = 0; ks < 4; ++ks) a = mfma32(*(LAS v4u*)(ka + T * 4096 + (((4 * ks + r) ^ c) * 16)), qf[ks], a);
      S[T] = a;
    }
#pragma unroll
    for (int U = 0; U < 2; ++U)
#pragma unroll
      for (int t = 0; t < 4; ++t) {
        const float lo = (jj & 1) ? S[4 * U + 1][t] : S[4 * U][t], hi = (jj & 1) ? S[4 * U + 3][t] : S[4 * U + 2][t];
        sc[4 * U + t] = (jj & 2) ? hi : lo;
      }
  }
  float mx = -INFINITY;
#pragma unroll
  for (int k = 0; k < 8; k += 2) {
    const unsigned u1 = pk2(sc[k], sc[k + 1]);
    const unsigned u2 = pk2(bflo(u1) * 0.08838834764831845f, bfhi(u1) * 0.08838834764831845f);
    sc[k] = bflo(u2); sc[k + 1] = bfhi(u2);
    mx = fmaxf(mx, fmaxf(sc[k], sc[k + 1]));
  }
  mx = fmaxf(mx, dpp<XOR1>(mx)); mx = fmaxf(mx, dpp<XOR2>(mx));
  mx = x32_max(x16_max(mx));
  if (lane < 16 && jj == 0) *(LAS float*)(L + L_MX + hd * 16 + kw * 4) = mx;
  DRAIN_LGKM();
  lds_add1(ct + C_MAX, lane);
  lds_wait_ge(ct + C_MAX, 4u);
  const f32x4 m4 = *(LAS f32x4*)(L + L_MX + hd * 16);
  const float m2 = fmaxf(fmaxf(m4.x, m4.y), fmaxf(m4.z, m4.w)) * 1.4426950408889634f;
  float ls = 0.f;
  {
    v4u P[2];
#pragma unroll
    for (int k = 0; k < 8; ++k) { const float p = __builtin_amdgcn_exp2f(fmaf(sc[k], 1.4426950408889634f, -m2)); ls += p; P[k >> 2][k & 3] = pk2(p, p); }
    *(LAS v4u*)(kb + lane * 16) = P[0]; *(LAS v4u*)(kb + 1024 + lane * 16) = P[1];
  }
  ls += dpp<XOR1>(ls); ls += dpp<XOR2>(ls);
  ls = x32_sum(x16_sum(ls));
  if (lane < 16 && jj == 0) *(LAS float*)(L + L_LX + hd * 16 + kw * 4) = ls;
  DRAIN_LGKM();
  lds_set1(ct + C_PRDY + kw);
  lds_add1(ct + C_PCNT, lane);
  if (kw == 0) {
    STAMP(6);
    lds_wait_ge(ct + C_PCNT, 4u);
    const f32x4 l4 = *(LAS f32x4*)(L + L_LX + hd * 16);
    const float lt = (l4.x + l4.y) + (l4.z + l4.w);
    if (lane < 16 && jj == 0) __builtin_amdgcn_raw_buffer_store_b64(v2u{__float_as_uint(m2), __float_as_uint(lt)}, rw, WS_ML + ((hi2 * 32 + 4 * grp + hd) * 8 + lo3) * 8, 0, AUX_SC1);
  }
  if (kw == 1) {
#pragma unroll 1
    for (unsigned n = 0; n < GSPIN; ++n) {
      const v4u a = __builtin_amdgcn_raw_buffer_load_b128(rw, WS_FLAG + lane * 16, 0, AUX_SC1), d = __builtin_amdgcn_raw_buffer_load_b128(rw, WS_FLAG + 1024 + lane * 16, 0, AUX_SC1);
      const unsigned bad = (a.x ^ tag.x) | (a.y ^ tag.y) | (a.z ^ tag.x) | (a.w ^ tag.y) | (d.x ^ tag.x) | (d.y ^ tag.y) | (d.z ^ tag.x) | (d.w ^ tag.y);
      if (__all(bad == 0)) break;
      MEMFENCE();
      __builtin_amdgcn_s_sleep(8);
    }
    lds_set1(ct + C_CERT);
    v4u g[8];
    const int j = min(c, 13);
#pragma unroll
    for (int k = 0; k < 8; ++k) g[k] = __builtin_amdgcn_raw_buffer_load_b128(rw, WS_GP + ((k * 4 + r) * 1792 + 56 * hh) * 4 + j * 16, 0, AUX_SC1);
    const f32x4 sg = (__builtin_bit_cast(f32x4, g[0]) + __builtin_bit_cast(f32x4, g[1])) + (__builtin_bit_cast(f32x4, g[2]) + __builtin_bit_cast(f32x4, g[3]));
    const f32x4 su = (__builtin_bit_cast(f32x4, g[4]) + __builtin_bit_cast(f32x4, g[5])) + (__builtin_bit_cast(f32x4, g[6]) + __builtin_bit_cast(f32x4, g[7]));
    v2u hv = {pk2(rbf(sg.x) * rbf(su.x), rbf(sg.y) * rbf(su.y)), pk2(rbf(sg.z) * rbf(su.z), rbf(sg.w) * rbf(su.w))};
    if (c >= 14) hv = v2u{0u, 0u};
    *(LAS v2u*)(L + L_HS + r * 192 + c * 8) = hv;
    DRAIN_LGKM();
    lds_add1(ct + C_WRDY, lane);
    STAMP(2);
  }
  const int sp = lane >> 5;
  float w0, w1, w2, w3;
  lds_wait_ge(ct + C_CERT, 1u);
  lds_wait_ge(ct + C_MLT, 1u);
  {
    v4u a, d;
    const int offm = WS_ML + ((kw * 32 + hh) * 8 + 4 * sp) * 8;
#pragma unroll 1
    for (unsigned n = 0; n < GSPIN; ++n) {
      a = __builtin_amdgcn_raw_buffer_load_b128(rw, offm, 0, AUX_SC1);
      d = __builtin_amdgcn_raw_buffer_load_b128(rw, offm + 16, 0, AUX_SC1);
      if (__all(max(umax4(a), umax4(d)) != SENT)) break;
      MEMFENCE();
      __builtin_amdgcn_s_sleep(4);
    }
    const float m0 = __uint_as_float(a.x), m1 = __uint_as_float(a.z), m2_ = __uint_as_float(d.x), m3 = __uint_as_float(d.z);
    const float M = x32_max(fmaxf(fmaxf(m0, m1), fmaxf(m2_, m3)));
    w0 = __builtin_amdgcn_exp2f(m0 - M); w1 = __builtin_amdgcn_exp2f(m1 - M); w2 = __builtin_amdgcn_exp2f(m2_ - M); w3 = __builtin_amdgcn_exp2f(m3 - M);
    const float iL = 1.f / x32_sum((w0 * __uint_as_float(a.y) + w1 * __uint_as_float(a.w)) + (w2 * __uint_as_float(d.y) + w3 * __uint_as_float(d.w)));
    w0 *= iL; w1 *= iL; w2 *= iL; w3 *= iL;
    KEEP(w0); KEEP(w1); KEEP(w2); KEEP(w3);
  }
  if (kw == 0) STAMP(7);
  if (kw >= 2) {
    const __amdgpu_buffer_rsrc_t ro = __builtin_amdgcn_make_buffer_rsrc((void*)wo, 0, 4194304, RSRC_W3);
    const __amdgpu_buffer_rsrc_t rd = __builtin_amdgcn_make_buffer_rsrc((void*)wd, 0, 1792 * 512 * 2, RSRC_W3);
    const int r8 = lane >> 3, c8 = lane & 7, hf = kw - 2;
    int vo[2], vd[4];
#pragma unroll
    for (int p = 0; p < 2; ++p) { vo[p] = (8 * (r8 & 3) + (r8 >> 2)) * 1024 + ((c8 ^ (2 * (((r8 >> 1) & 1) | (p << 1)))) * 16); KEEP(vo[p]); }
#pragma unroll
    for (int ii = 0; ii < 4; ++ii) {
      const int pos = 56 * hh + 8 * (4 * hf + ii) + r8, row = (pos & ~63) + pos2col(pos & 63);
      vd[ii] = row * 1024 + ((c8 ^ (2 * (((r8 >> 1) & 1) | ((ii & 1) << 1)))) * 16); KEEP(vd[ii]);
    }
    lds_wait_ge(ct + C_VISS, 4u);
#pragma unroll
    for (int ii = 0; ii < 4; ++ii)
      if (4 * hf + ii < 7) __builtin_amdgcn_raw_ptr_buffer_load_lds(rd, (LAS void*)(L + L_WD + (4 * hf + ii) * 1024), 16, vd[ii], lo3 * 128, 0, AUX_NT);
#pragma unroll
    for (int ii = 0; ii < 8; ++ii)
      __builtin_amdgcn_raw_ptr_buffer_load_lds(ro, (LAS void*)(L + L_WO + (8 * hf + ii) * 1024), 16, vo[ii & 1], (hh * 128 + 32 * ((ii >> 1) & 3) + 4 * hf + 2 * (ii & 1)) * 1024 + lo3 * 128, 0, AUX_NT);
  }
  SB();
  int off = WS_O + ((kw * 32 + hh) * 8 + 4 * sp) * 512 + (lane & 31) * 16;
  int offp = lane < 16 ? WS_P2 + (lo3 * 32 + hh) * 1024 + (16 * kw + lane) * 16 : 0x7fffffff;
  unsigned ta = (unsigned)(size_t)L + L_WO + (8 * r + hd) * 128 + ((kw ^ ((hd >> 1) | ((r & 1) << 1))) * 32) + jj * 8;
  unsigned aw = (unsigned)(size_t)L + L_OS + kw * 320 + (lane & 31) * 8 + sp * 4, ar = (unsigned)(size_t)L + L_OS + jj * 320 + r * 16, ah = (unsigned)(size_t)L + L_HS + jj * 192 + r * 16;
  KEEP(off); KEEP(offp); KEEP(ta); KEEP(aw); KEEP(ar); KEEP(ah);
  lds_wait_ge(ct + C_PUB, 1u);
  if (PDELAY) __builtin_amdgcn_s_sleep(PDELAY);
  if (kw == 0) STAMP(11);
  v4u o[4];
#pragma unroll
  for (int k = 0; k < 4; ++k) o[k] = __builtin_amdgcn_raw_buffer_load_b128(rw, off + k * 512, 0, AUX_SC1);
  SB();
  if (kw >= 2) { asm volatile("s_waitcnt vmcnt(4)" ::: "memory"); lds_add1(ct + C_WRDY, lane); }
  lds_wait_ge(ct + C_WRDY, 3u);
  v4u bf[4];
  f32x4 acc;
  {
    v2u t[12]; v4u h0, h1;
    asm volatile("ds_read_b128 %12, %15\n\tds_read_b64_tr_b16 %8, %14 offset:16384\n\tds_read_b64_tr_b16 %9, %14 offset:16896\n\t"
                 "ds_read_b128 %13, %15 offset:64\n\tds_read_b64_tr_b16 %10, %14 offset:20480\n\tds_read_b64_tr_b16 %11, %14 offset:20992\n\t"
                 "ds_read_b64_tr_b16 %0, %14\n\tds_read_b64_tr_b16 %1, %14 offset:512\n\tds_read_b64_tr_b16 %2, %14 offset:4096\n\tds_read_b64_tr_b16 %3, %14 offset:4608\n\t"
                 "ds_read_b64_tr_b16 %4, %14 offset:8192\n\tds_read_b64_tr_b16 %5, %14 offset:8704\n\tds_read_b64_tr_b16 %6, %14 offset:12288\n\tds_read_b64_tr_b16 %7, %14 offset:12800\n\t"
                 "s_waitcnt lgkmcnt(0)"
                 : "=&v"(t[0]), "=&v"(t[1]), "=&v"(t[2]), "=&v"(t[3]), "=&v"(t[4]), "=&v"(t[5]), "=&v"(t[6]), "=&v"(t[7]), "=&v"(t[8]), "=&v"(t[9]), "=&v"(t[10]), "=&v"(t[11]), "=&v"(h0), "=&v"(h1)
                 : "v"(ta), "v"(ah) : "memory");
    acc = mfma32(h0, v4u{t[8].x, t[8].y, t[9].x, t[9].y}, f32x4{0, 0, 0, 0});
    acc = mfma32(h1, v4u{t[10].x, t[10].y, t[11].x, t[11].y}, acc);
#pragma unroll
    for (int ks = 0; ks < 4; ++ks) { bf[ks] = v4u{t[2 * ks].x, t[2 * ks].y, t[2 * ks + 1].x, t[2 * ks + 1].y}; KEEP(bf[ks]); }
    KEEP(acc);
  }
  SB();
#pragma unroll 1
  for (unsigned n = 0; n < GSPIN; ++n) {
    if (__all(umax3(umax3(umax3(o[0].x, o[0].y, o[0].z), umax3(o[0].w, o[1].x, o[1].y), umax3(o[1].z, o[1].w, o[2].x)), umax3(umax3(o[2].y, o[2].z, o[2].w), umax3(o[3].x, o[3].y, o[3].z), o[3].w), 0u) != SENT)) break;
    MEMFENCE();
#pragma unroll
    for (int k = 0; k < 4; ++k) o[k] = __builtin_amdgcn_raw_buffer_load_b128(rw, off + k * 512, 0, AUX_SC1);
  }
  if (kw == 0) STAMP(12);
  {
    float y[4];
#pragma unroll
    for (int t = 0; t < 4; ++t) y[t] = fmaf(w3, __uint_as_float(o[3][t]), fmaf(w2, __uint_as_float(o[2][t]), fmaf(w1, __uint_as_float(o[1][t]), w0 * __uint_as_float(o[0][t]))));
    auto e0 = __builtin_amdgcn_permlane32_swap(__float_as_uint(y[0]), __float_as_uint(y[2]), false, false);
    auto e1 = __builtin_amdgcn_permlane32_swap(__float_as_uint(y[1]), __float_as_uint(y[3]), false, false);
    const unsigned ob = pk2(__uint_as_float(e0[0]) + __uint_as_float(e0[1]), __uint_as_float(e1[0]) + __uint_as_float(e1[1]));
    v4u af[4];
    asm volatile("ds_write_b32 %4, %5\n\ts_waitcnt lgkmcnt(0)\n\ts_barrier\n\t"
                 "ds_read_b128 %0, %6\n\tds_read_b128 %1, %6 offset:64\n\tds_read_b128 %2, %6 offset:128\n\tds_read_b128 %3, %6 offset:192\n\ts_waitcnt lgkmcnt(0)"
                 : "=&v"(af[0]), "=&v"(af[1]), "=&v"(af[2]), "=&v"(af[3]) : "v"(aw), "v"(ob), "v"(ar) : "memory");
#pragma unroll
    for (int ks = 0; ks < 4; ++ks) acc = mfma32(af[ks], bf[ks], acc);
    __builtin_amdgcn_raw_buffer_store_b128(__builtin_bit_cast(v4u, acc), rw, offp, 0, AUX_SC1);
  }
  if (kw == 0) STAMP(13);
}

extern "C" void kernel_launch(void* const* d_in, const int* in_sizes, int n_in, void* d_out, int out_size, void* d_ws, size_t ws_size, hipStream_t stream) {
  fused_decode<<<NWG, NTHR, 0, stream>>>((const char*)d_in[0], (const char*)d_in[1], (const char*)d_in[2], (const char*)d_in[3], (const char*)d_in[4],
                                         (const char*)d_in[6], (const char*)d_in[7], (const char*)d_in[8], (char*)d_out, (char*)d_ws);
}
```

```cpp
#include <hip/hip_runtime.h>
#include <hip/hip_bf16.h>
#include <stdint.h>

#define LAS __attribute__((address_space(3)))
typedef unsigned v4u __attribute__((ext_vector_type(4)));
typedef unsigned v2u __attribute__((ext_vector_type(2)));
typedef float f32x4 __attribute__((ext_vector_type(4)));
typedef float f32x2 __attribute__((ext_vector_type(2)));
typedef short s16x4 __attribute__((ext_vector_type(4)));
typedef __bf16 bf16x8 __attribute__((ext_vector_type(8)));
typedef __bf16 bf16x2 __attribute__((ext_vector_type(2)));
typedef unsigned long long u64;

constexpr int TAUK16 = 230;
constexpr int TAUV16 = 228;
constexpr int KE = 4;
constexpr int TW = 55;
constexpr int WPACE16 = 90;
constexpr int TK1 = 165;
constexpr int TV0 = TK1 + (((32 - KE) * TAUK16) >> 4);
constexpr int TPT = 300;
constexpr int TA = TV0 - 130;
constexpr int TQ = TV0 - 30;
constexpr int TG = TV0 + 150;
constexpr int ANC = 32;
constexpr int TWO = 34;
constexpr int WOPACE16 = 72;
constexpr int T1 = 134;
constexpr int T2 = 226;
constexpr int PD2 = 12;
constexpr int NCAP = 12;
constexpr int PV0 = 22, PVN = 3, PVLAG = 10;

constexpr int NWG = 256, NTHR = 640;
constexpr int AUX_NT = 2, AUX_SC1 = 16, RSRC_W3 = 0x00020000, OOB = 0x7fffffff;
constexpr int CODE_BYTES = 17408;
constexpr int WS_EPOCH = 0, WS_ST = 1024, WS_ML = 4096, WS_QP = WS_ML + 4 * 32 * 128, WS_GP = WS_QP + 4 * 4 * 4096 * 4,
              WS_O = WS_GP + 2 * 4 * 4 * 1792 * 4, WS_P2 = WS_O + 4 * 32 * 8 * 512, WS_END = WS_P2 + 4 * 64 * 2048;
constexpr int XS_WAVE = 16 * 80 + 1024;
constexpr int L_K = 0, L_WO = 131072, L_WD = L_WO + 16384, L_QS = L_WD + 8192, L_OS = L_QS + 1280, L_HS = L_OS + 768, L_XS = L_HS + 256,
              L_MX = L_XS + 2 * XS_WAVE, L_LX = L_MX + 64, L_CT = L_LX + 64, L_END = L_CT + 64;
enum { C_QRDY = 0, C_MAX = 1, C_PRDY = 2  , C_PCNT = 6, C_MRG = 7, C_PUB = 8, C_WRDY = 9, C_ANC = 10, C_ARDY = 11, C_X0 = 12, C_X1 = 13, C_ORDY = 14 };
static_assert(C_ORDY == C_WRDY + 5 && C_ORDY == C_PUB + 6, "immediate offsets in the asm statements below");
constexpr unsigned GSPIN = 1u << 16, LSPIN = 1u << 23;
constexpr float LOG2E = 1.4426950408889634f;

__device__ __forceinline__ float bflo(unsigned u) { return __builtin_bit_cast(float, u << 16); }
__device__ __forceinline__ float bfhi(unsigned u) { return __builtin_bit_cast(float, u & 0xffff0000u); }
__device__ __forceinline__ unsigned pk2(float lo, float hi) { f32x2 f = {lo, hi}; return __builtin_bit_cast(unsigned, __builtin_convertvector(f, bf16x2)); }
__device__ __forceinline__ float rbf(float f) { return bflo(pk2(f, f)); }
template <int CTRL> __device__ __forceinline__ float dpp(float x) { return __builtin_bit_cast(float, __builtin_amdgcn_mov_dpp(__builtin_bit_cast(int, x), CTRL, 0xf, 0xf, true)); }
template <int CTRL> __device__ __forceinline__ unsigned dppu(unsigned x) { return (unsigned)__builtin_amdgcn_mov_dpp((int)x, CTRL, 0xf, 0xf, true); }
constexpr int XOR1 = 0xB1, XOR2 = 0x4E, HMIR = 0x141, ROR8 = 0x128, ROR4 = 0x124;
__device__ __forceinline__ float row16_sum(float x) { x += dpp<XOR1>(x); x += dpp<XOR2>(x); x += dpp<HMIR>(x); x += dpp<ROR8>(x); return x; }
__device__ __forceinline__ float add16(float a, float b) { auto s = __builtin_amdgcn_permlane16_swap(__float_as_uint(a), __float_as_uint(b), false, false); return __uint_as_float(s[0]) + __uint_as_float(s[1]); }
__device__ __forceinline__ float add32(float a, float b) { auto s = __builtin_amdgcn_permlane32_swap(__float_as_uint(a), __float_as_uint(b), false, false); return __uint_as_float(s[0]) + __uint_as_float(s[1]); }
__device__ __forceinline__ float x16_max(float x) { auto s = __builtin_amdgcn_permlane16_swap(__float_as_uint(x), __float_as_uint(x), false, false); return fmaxf(__uint_as_float(s[0]), __uint_as_float(s[1])); }
__device__ __forceinline__ float x32_max(float x) { auto s = __builtin_amdgcn_permlane32_swap(__float_as_uint(x), __float_as_uint(x), false, false); return fmaxf(__uint_as_float(s[0]), __uint_as_float(s[1])); }
__device__ __forceinline__ f32x4 mfma16(v2u a, v2u b, f32x4 c) { return __builtin_amdgcn_mfma_f32_16x16x16bf16_1k(__builtin_bit_cast(s16x4, a), __builtin_bit_cast(s16x4, b), c, 0, 0, 0); }
__device__ __forceinline__ f32x4 mfma32(v4u a, v4u b, f32x4 c) { return __builtin_amdgcn_mfma_f32_16x16x32_bf16(__builtin_bit_cast(bf16x8, a), __builtin_bit_cast(bf16x8, b), c, 0, 0, 0); }
__device__ __forceinline__ v4u enc4(f32x4 v, unsigned t8) { const v4u u = __builtin_bit_cast(v4u, v); return v4u{(u.x & ~0xffu) | t8, (u.y & ~0xffu) | t8, (u.z & ~0xffu) | t8, (u.w & ~0xffu) | t8}; }
__device__ __forceinline__ v4u dec4(v4u u, unsigned t8) { return v4u{u.x ^ t8, u.y ^ t8, u.z ^ t8, u.w ^ t8}; }
__device__ __forceinline__ unsigned or4(v4u u) { return (u.x | u.y | u.z) | u.w; }
__device__ __forceinline__ f32x4 asf(v4u u) { return __builtin_bit_cast(f32x4, u); }
__device__ __forceinline__ __amdgpu_buffer_rsrc_t rsrc(const void* p, int bytes) { return __builtin_amdgcn_make_buffer_rsrc((void*)p, 0, bytes, RSRC_W3); }
#define DRAIN_VM() asm volatile("s_waitcnt vmcnt(0)" ::: "memory")
#define DRAIN_LGKM() asm volatile("s_waitcnt lgkmcnt(0)" ::: "memory")
#define CAP() asm volatile("s_waitcnt vmcnt(%0)" :: "i"(NCAP - 1) : "memory")
#define MEMFENCE() asm volatile("" ::: "memory")
#define SB() __builtin_amdgcn_sched_barrier(0)
#define KEEP(x) asm volatile("" : "+v"(x))

__device__ __forceinline__ unsigned now32() { return (unsigned)__builtin_amdgcn_s_memrealtime(); }
__device__ __forceinline__ void pace(unsigned t) { while ((int)(now32() - t) < 0) {} }
__device__ __forceinline__ void pace_long(unsigned t) { while ((int)(now32() - t) < 0) __builtin_amdgcn_s_sleep(1); }
__device__ __forceinline__ unsigned lds_get(LAS unsigned* p) { unsigned v; asm volatile("ds_read_b32 %0, %1\n\ts_waitcnt lgkmcnt(0)" : "=v"(v) : "v"((unsigned)(size_t)p) : "memory"); return __builtin_amdgcn_readfirstlane(v); }
__device__ __forceinline__ void lds_wait_ge(LAS unsigned* p, unsigned want) {
#pragma unroll 1
  for (unsigned n = 0; n < LSPIN; ++n) {
    if (lds_get(p) >= want) break;
    __builtin_amdgcn_s_sleep(1);
  }
}
__device__ __forceinline__ void lds_set(LAS unsigned* p, unsigned v) { asm volatile("ds_write_b32 %0, %1" :: "v"((unsigned)(size_t)p), "v"(v) : "memory"); }
__device__ __forceinline__ void lds_add1(LAS unsigned* p, int lane) { if (lane == 0) asm volatile("ds_add_u32 %0, %1" :: "v"((unsigned)(size_t)p), "v"(1u) : "memory"); }

__device__ __forceinline__ int pos2col(int pos) { return 32 * ((pos >> 4) & 3) + 8 * (pos & 3) + 4 * (pos >> 6) + ((pos >> 2) & 3); }

__device__ __forceinline__ void ln_tile(const v4u (&xr)[4], const v4u (&w)[16], LAS char* xs, int lane, int hf, int rot, f32x4& d0, f32x4& d1) {
  float e[32];
#pragma unroll
  for (int k = 0; k < 4; ++k)
#pragma unroll
    for (int j = 0; j < 4; ++j) { e[8 * k + 2 * j] = bflo(xr[k][j]); e[8 * k + 2 * j + 1] = bfhi(xr[k][j]); }
  float s0 = 0.f, s1 = 0.f;
#pragma unroll
  for (int k = 0; k < 16; ++k) { s0 += e[2 * k]; s1 += e[2 * k + 1]; }
  const float mean = row16_sum(s0 + s1) * (1.f / 512.f);
  float q0 = 0.f, q1 = 0.f;
#pragma unroll
  for (int k = 0; k < 16; ++k) { float a = e[2 * k] - mean, b = e[2 * k + 1] - mean; q0 = fmaf(a, a, q0); q1 = fmaf(b, b, q1); }
  const float var = rbf(row16_sum(q0 + q1) * (1.f / 512.f));
  const float mb = rbf(mean), den = rbf(sqrtf(rbf(var + 1e-5f))), inv = 1.f / den;
  const int g = lane >> 4, c = lane & 15, bb = (lane >> 2) & 3, jj = lane & 3;
  if ((c >> 3) == hf) {
#pragma unroll
    for (int j = 0; j < 4; ++j) {
      const unsigned u = pk2(rbf(e[j] - mb) * inv, rbf(e[j + 4] - mb) * inv);
      *(LAS unsigned*)(xs + (g * 4 + j) * 80 + (c & 7) * 4) = u; *(LAS unsigned*)(xs + (g * 4 + j) * 80 + (c & 7) * 4 + 32) = u;
    }
  }
  DRAIN_LGKM();
  unsigned pr[8];
#pragma unroll
  for (int i = 0; i < 8; ++i) pr[i] = *(LAS unsigned*)(xs + (bb * 4 + g) * 80 + rot * 2 + 4 * i);
  SB();
  const unsigned z = 0x0c0c0c0cu;
  const unsigned sA0 = jj == 0 ? 0x0c0c0100u : jj == 1 ? 0x01000c0cu : z, sB0 = jj == 2 ? 0x0c0c0100u : jj == 3 ? 0x01000c0cu : z;
  const unsigned sA1 = jj == 0 ? 0x0c0c0302u : jj == 1 ? 0x03020c0cu : z, sB1 = jj == 2 ? 0x0c0c0302u : jj == 3 ? 0x03020c0cu : z;
  d0 = f32x4{0, 0, 0, 0}; d1 = d0;
#pragma unroll
  for (int i = 0; i < 16; ++i) {
    const unsigned p = pr[i >> 1];
    const v2u b = {__builtin_amdgcn_perm(p, p, (i & 1) ? sA1 : sA0), __builtin_amdgcn_perm(p, p, (i & 1) ? sB1 : sB0)};
    d0 = mfma16(v2u{w[i].x, w[i].y}, b, d0); d1 = mfma16(v2u{w[i].z, w[i].w}, b, d1);
  }
}

constexpr int pv_done(int i) { return i < PV0 ? 0 : (PVN * (i - PV0 + 1) < i + 1 - PVLAG ? PVN * (i - PV0 + 1) : i + 1 - PVLAG); }
template <int J> __device__ __forceinline__ void pv_step(const v4u& v, const v4u& P0, const v4u& P1, unsigned mk0, unsigned mk1, f32x4& a0, f32x4& a1) {
  const unsigned s = (J < 16 ? P0 : P1)[J & 3];
  const v2u p = {dppu<0x55 * ((J >> 2) & 3)>(s) & mk0, dppu<0x55 * ((J >> 2) & 3)>(s) & mk1};
  a0 = mfma16(v2u{v.x, v.y}, p, a0); a1 = mfma16(v2u{v.z, v.w}, p, a1);
}
template <int LO, int HI> __device__ __forceinline__ void pv_range(const v4u (&v)[32], const v4u& P0, const v4u& P1, unsigned mk0, unsigned mk1, f32x4& a0, f32x4& a1) {
  if constexpr (LO < HI) { pv_step<LO>(v[LO], P0, P1, mk0, mk1, a0, a1); pv_range<LO + 1, HI>(v, P0, P1, mk0, mk1, a0, a1); }
}

#define KVOFF(i, w) (((((i) + pair) & 31) << 15) + (((lo3 + (i)) & 1) << 10) + (((lo3 + (i)) & 6) << 12) + ((w) << 11))
#define VSLOT(s) (tv + (((s) * TAUV16) >> 4))
#define GET_TV() lds_wait_ge(ct + C_ARDY, 1u); const unsigned tv = lds_get(ct + C_ANC) + TV0

__global__ void __launch_bounds__(NTHR, 3)
fused_decode(const char* __restrict__ Kc, const char* __restrict__ x, const char* __restrict__ wq, const char* __restrict__ wg, const char* __restrict__ wu, char* __restrict__ ws,
             const char* __restrict__ Vc, char* __restrict__ out, const char* __restrict__ wo, const char* __restrict__ wd) {
  __shared__ __attribute__((aligned(16))) char smem_[L_END];
  LAS char* L = (LAS char*)smem_;
  LAS unsigned* ct = (LAS unsigned*)(L + L_CT);
  const int lane = threadIdx.x & 63, wave = __builtin_amdgcn_readfirstlane(threadIdx.x >> 6), blk = blockIdx.x;
  const int grp = blk & 7, mem = blk >> 3, hi2 = mem >> 3, lo3 = mem & 7;
  const int hh = 4 * grp + hi2, pair = hi2 * 8 + grp;
  const int part = lo3 & 1, cq = lo3 >> 1, t2 = 2 * hh + part;
  if (wave == 0) {
    u64 pc; asm volatile("s_getpc_b64 %0" : "=s"(pc));
    __builtin_amdgcn_raw_ptr_buffer_load_lds(rsrc((void*)(pc & ~255ull), CODE_BYTES), (LAS void*)(L + L_WO), 16, lane * 16, mem * 1024, 0, 0);
  }
  if (wave == 9) { if (lane < 16) ct[lane] = 0u; }
  int vk = (lane >> 4) * 256 + (((lane & 15) ^ (4 * (lane >> 4))) * 16);
  const bool isk = wave >= 2 && wave < 6;
  int so0 = KVOFF(0, wave - 2), pk0 = pair << 20, lk0 = (int)(size_t)(L + L_K) + (wave - 2) * 32768;
  KEEP(vk); asm volatile("" : "+s"(so0), "+s"(pk0), "+s"(lk0));
  asm volatile("" :: "s"(x), "s"(Kc), "s"(Vc), "s"(wq), "s"(wo), "s"(wg), "s"(wu), "s"(wd), "s"(out), "s"(ws));
  const __amdgpu_buffer_rsrc_t rk = rsrc(Kc + (unsigned)pk0, 1048576);
  if (isk) __builtin_amdgcn_raw_ptr_buffer_load_lds(rk, (LAS void*)(size_t)lk0, 16, vk, so0, 0, AUX_NT);
  SB();
  const unsigned tb = now32();
  const __amdgpu_buffer_rsrc_t rw = rsrc(ws, WS_END);
  const int r = lane >> 4, c = lane & 15, hd = (lane >> 2) & 3, jj = lane & 3;
  DRAIN_LGKM();
  __builtin_amdgcn_s_barrier();
  if (isk) {
    const int kw = wave - 2;
    unsigned t16 = TAUK16;
#pragma unroll 1
    for (int j = 1; j < 32; ++j) {
      if (j == KE) t16 = TK1 * 16;
      const int vo_ = vk ^ ((j & 3) << 4), so_ = KVOFF(j, kw);
      pace(tb + (t16 >> 4));
      __builtin_amdgcn_raw_ptr_buffer_load_lds(rk, (LAS void*)(size_t)(lk0 + j * 1024), 16, vo_, so_, 0, AUX_NT);
      CAP();
      t16 += TAUK16;
    }
  }

  if (wave < 2) {
    const __amdgpu_buffer_rsrc_t rx = rsrc(x, 4096);
    LAS char* xs = L + L_XS + wave * XS_WAVE;
    const bool tq = mem < 16, have = mem < 30, isq = wave == 0;
    const int tt = (mem - 16) * 8 + grp, cgg = tt >> 3, which = (tt >> 2) & 1;
    const int rs = tq ? mem >> 2 : tt & 3, pitch = tq ? 8192 : 3584, cb = tq ? (4 * grp + (mem & 3)) * 256 : cgg * 256, rot = tq ? 4 * rs + 2 * wave : 0;
    const __amdgpu_buffer_rsrc_t rq = rsrc(tq ? wq : which ? wu : wg, have ? 512 * pitch : 0);
    v4u xr[4], w[16];
#pragma unroll
    for (int k = 0; k < 4; ++k) xr[k] = __builtin_amdgcn_raw_buffer_load_b128(rx, r * 1024 + c * 16, ((rs + k) & 3) * 256, 0);
    const unsigned epoch = __builtin_amdgcn_raw_buffer_load_b32(rw, WS_EPOCH, 0, AUX_SC1);
    SB();
#pragma unroll
    for (int i = 0; i < 16; ++i) {
      if (!(i & 1)) pace(tb + TW + ((i * WPACE16) >> 4));
      w[i] = __builtin_amdgcn_raw_buffer_load_b128(rq, r * pitch + c * 16, (rs * 128 + 64 * wave + 4 * ((i + rot) & 15)) * pitch + cb, AUX_NT);
      if (i & 1) SB();
    }
    f32x4 d0, d1;
    ln_tile(xr, w, xs, lane, wave, rot, d0, d1);
    *(LAS f32x4*)(xs + 16 * 80 + lane * 16) = isq ? d1 : d0;
    lds_set(ct + C_X0 + wave, 1u);
    lds_wait_ge(ct + C_X1 - wave, 1u);
    const f32x4 dx = *(LAS f32x4*)(L + L_XS + (1 - wave) * XS_WAVE + 16 * 80 + lane * 16);
    const f32x4 y = isq ? d0 + dx : dx + d1;
    const unsigned t8 = (epoch + 1u) & 0xffu;
    const int po = tq ? WS_QP + ((rs * 4 + hd) * 4096) * 4 + 2 * cb : WS_GP + (((which * 4 + rs) * 4 + hd) * 1792) * 4 + 2 * cb;
    __builtin_amdgcn_raw_buffer_store_b128(enc4(y, t8), rw, have ? po + wave * 256 + r * 64 + jj * 16 : OOB, 0, AUX_SC1);
    const int go = isq ? WS_QP + (hi2 * 4096 + grp * 512) * 4 + lane * 16 : WS_GP + (r * 1792 + 28 * t2) * 4 + min(c, 6) * 16;
    const int gs = isq ? 4 * 4096 * 4 : 4 * 1792 * 4, gh = isq ? 1024 : 16 * 1792 * 4;
    pace_long(tb + (isq ? TQ : TG));
    v4u g[8];
#pragma unroll 1
    for (unsigned n = 0; n < GSPIN; ++n) {
      unsigned bad = 0;
#pragma unroll
      for (int k = 0; k < 8; ++k) g[k] = __builtin_amdgcn_raw_buffer_load_b128(rw, go, (k & 3) * gs + (k >> 2) * gh, AUX_SC1);
#pragma unroll
      for (int k = 0; k < 8; ++k) { g[k] = dec4(g[k], t8); bad |= or4(g[k]); }
      if (__all((bad & 0xffu) == 0u)) break;
      MEMFENCE();
      __builtin_amdgcn_s_sleep(8);
    }
    const f32x4 sa = (asf(g[0]) + asf(g[1])) + (asf(g[2]) + asf(g[3])), sb = (asf(g[4]) + asf(g[5])) + (asf(g[6]) + asf(g[7]));
    if (isq) {
#pragma unroll
      for (int u = 0; u < 2; ++u) {
        const f32x4 sq = u ? sb : sa;
        LAS char* q = L + L_QS + (2 * u + (lane >> 5)) * 320 + pos2col(4 * (lane & 31)) * 2;
        const unsigned a = pk2(sq.x, sq.y), d = pk2(sq.z, sq.w);
        *(LAS unsigned short*)(q) = (unsigned short)a; *(LAS unsigned short*)(q + 16) = (unsigned short)(a >> 16);
        *(LAS unsigned short*)(q + 32) = (unsigned short)d; *(LAS unsigned short*)(q + 48) = (unsigned short)(d >> 16);
      }
      lds_set(ct + C_QRDY, 1u);
    } else {
      v2u hv = {pk2(rbf(sa.x) * rbf(sb.x), rbf(sa.y) * rbf(sb.y)), pk2(rbf(sa.z) * rbf(sb.z), rbf(sa.w) * rbf(sb.w))};
      if (c >= 7) hv = v2u{0u, 0u};
      if (c < 8) *(LAS v2u*)(L + L_HS + r * 64 + c * 8) = hv;
    }
    const __amdgpu_buffer_rsrc_t ro = rsrc(wo, 4194304), rd = rsrc(wd, 1792 * 1024);
    const int wr = 2 * (hh & 3);
    int vo[2], vd[4];
#pragma unroll
    for (int p = 0; p < 2; ++p) { vo[p] = 8 * r * 1024 + ((c ^ (2 * (r | (((p + (wr >> 1)) & 1) << 2)))) * 16); KEEP(vo[p]); }
#pragma unroll
    for (int ii = 0; ii < 4; ++ii) {
      const int pos = 28 * t2 + 4 * (4 * wave + ii) + r, row = (pos & ~127) + pos2col(pos & 127);
      vd[ii] = 4 * wave + ii < 7 ? row * 1024 + ((c ^ (2 * (r | (((ii >> 1) & 1) << 2)))) * 16) : OOB;
      KEEP(vd[ii]);
    }
    DRAIN_LGKM();
    GET_TV();
    pace_long(VSLOT(31) + TWO - 10);
#pragma unroll
    for (int ii = 0; ii < 12; ++ii) {
      pace(VSLOT(31) + TWO + ((ii * WOPACE16) >> 4));
      if (ii < 4) __builtin_amdgcn_raw_ptr_buffer_load_lds(rd, (LAS void*)(L + L_WD + (4 * wave + ii) * 1024), 16, vd[ii], cq * 256, 0, AUX_NT);
      else { const int j = 8 * wave + ((ii - 4 + wr) & 7);
        __builtin_amdgcn_raw_ptr_buffer_load_lds(ro, (LAS void*)(L + L_WO + j * 1024), 16, vo[((ii - 4) >> 1) & 1], (hh * 128 + 32 * (j >> 2) + 4 * part + (j & 3)) * 1024 + cq * 256, 0, AUX_NT); }
      SB();
    }
    asm volatile("s_waitcnt vmcnt(8)\n\ts_mov_b64 exec, 1\n\tds_add_u32 %0, %1\n\ts_waitcnt vmcnt(0)\n\tds_add_u32 %0, %1 offset:20\n\ts_mov_b64 exec, -1"
                 :: "v"((unsigned)(size_t)(ct + C_WRDY)), "v"(1u) : "memory");
    return;
  }

  if (wave < 6) {
    const int kw = wave - 2;
    LAS char* kb = L + L_K + kw * 32768;
    unsigned tag = __builtin_amdgcn_raw_buffer_load_b32(rw, WS_EPOCH, 0, AUX_SC1);
    lds_wait_ge(ct + C_QRDY, 1u);
    DRAIN_VM();
    tag += 1u;
    unsigned t8 = tag & 0xffu;
    float sc[8];
    {
      v4u qf[4];
#pragma unroll
      for (int ks = 0; ks < 4; ++ks) qf[ks] = *(LAS v4u*)(L + L_QS + hd * 320 + ks * 64 + r * 16);
      f32x4 S[8];
      LAS char* ka = kb + (4 * (c & 3) + (c >> 2)) * 256;
      v4u kf[2][8];
#define KREAD(B, T0) _Pragma("unroll") for (int i = 0; i < 8; ++i) kf[B][i] = *(LAS v4u*)(ka + ((T0) + (i >> 2)) * 4096 + (((4 * (i & 3) + r) ^ c) * 16))
#define KMMA(B, T0) _Pragma("unroll") for (int i = 0; i < 8; ++i) { const int ks = i >> 1, T = (T0) + (i & 1); S[T] = mfma32(kf[B][4 * (i & 1) + ks], qf[ks], ks ? S[T] : f32x4{0, 0, 0, 0}); }
      KREAD(0, 0); KREAD(1, 2); SB();
      KMMA(0, 0); SB(); KREAD(0, 4); SB();
      KMMA(1, 2); SB(); KREAD(1, 6); SB();
      KMMA(0, 4); SB();
      KMMA(1, 6); SB();
#pragma unroll
      for (int U = 0; U < 2; ++U)
#pragma unroll
        for (int t = 0; t < 4; ++t) {
          const float lo = (jj & 1) ? S[4 * U + 1][t] : S[4 * U][t], hi = (jj & 1) ? S[4 * U + 3][t] : S[4 * U + 2][t];
          sc[4 * U + t] = (jj & 2) ? hi : lo;
        }
    }
    float mx = -INFINITY;
#pragma unroll
    for (int k = 0; k < 8; k += 2) {
      const unsigned u1 = pk2(sc[k], sc[k + 1]);
      const unsigned u2 = pk2(bflo(u1) * 0.08838834764831845f, bfhi(u1) * 0.08838834764831845f);
      sc[k] = bflo(u2); sc[k + 1] = bfhi(u2);
      mx = fmaxf(mx, fmaxf(sc[k], sc[k + 1]));
    }
    mx = fmaxf(mx, dpp<XOR1>(mx)); mx = fmaxf(mx, dpp<XOR2>(mx));
    mx = x32_max(x16_max(mx));
    if (lane < 16 && jj == 0) *(LAS float*)(L + L_MX + hd * 16 + kw * 4) = mx;
    lds_add1(ct + C_MAX, lane);
    lds_wait_ge(ct + C_MAX, 4u);
    const f32x4 m4 = *(LAS f32x4*)(L + L_MX + hd * 16);
    const float mw = fmaxf(fmaxf(m4.x, m4.y), fmaxf(m4.z, m4.w));
    float ls = 0.f;
    {
      v4u P[2];
#pragma unroll
      for (int k = 0; k < 8; ++k) { const float p = __builtin_amdgcn_exp2f((sc[k] - mw) * LOG2E); ls += p; P[k >> 2][k & 3] = pk2(p, p); }
      *(LAS v4u*)(kb + lane * 16) = P[0]; *(LAS v4u*)(kb + 1024 + lane * 16) = P[1];
      lds_set(ct + C_PRDY + kw, 1u);
    }
    ls += dpp<XOR1>(ls); ls += dpp<XOR2>(ls);
    ls = add16(ls, ls); ls = add32(ls, ls);
    if (lane < 16 && jj == 0) *(LAS float*)(L + L_LX + hd * 16 + kw * 4) = ls;
    lds_add1(ct + C_PCNT, lane);
    if (kw == 0) {
      lds_wait_ge(ct + C_PCNT, 4u);
      const f32x4 l4 = *(LAS f32x4*)(L + L_LX + hd * 16);
      const float lt = (l4.x + l4.y) + (l4.z + l4.w);
      __builtin_amdgcn_raw_buffer_store_b128(v4u{__float_as_uint(mw), tag, __float_as_uint(lt), tag}, rw,
                                             (lane < 16 && jj == 0) ? WS_ML + ((hi2 * 32 + 4 * grp + hd) * 8 + lo3) * 16 : OOB, 0, AUX_SC1);
    }
    float w0, w1;
    GET_TV();
    pace_long(VSLOT(32));
    {
      v4u a;
      const int offm = WS_ML + ((kw * 32 + hh) * 8 + (lane & 7)) * 16;
#pragma unroll 1
      for (unsigned n = 0; n < GSPIN; ++n) {
        a = __builtin_amdgcn_raw_buffer_load_b128(rw, offm, 0, AUX_SC1);
        if (__all(a.y == tag && a.w == tag)) break;
        MEMFENCE();
        __builtin_amdgcn_s_sleep(4);
      }
      const float m = __uint_as_float(a.x);
      float M = fmaxf(m, dpp<XOR1>(m)); M = fmaxf(M, dpp<XOR2>(M)); M = fmaxf(M, dpp<HMIR>(M));
      const float w = __builtin_amdgcn_exp2f((m - M) * LOG2E);
      float Ls = w * __uint_as_float(a.z); Ls += dpp<XOR1>(Ls); Ls += dpp<XOR2>(Ls); Ls += dpp<HMIR>(Ls);
      const int wn = __float_as_int(w / Ls);
      w0 = __int_as_float(__builtin_amdgcn_ds_bpermute(8 * r, wn)); w1 = __int_as_float(__builtin_amdgcn_ds_bpermute(8 * r + 4, wn));
      KEEP(w0); KEEP(w1);
    }
    int off = WS_O + ((kw * 32 + hh) * 8 + 2 * r) * 512 + part * 256 + c * 16;
    int offp = lane < 32 ? WS_P2 + (cq * 64 + t2) * 2048 + (32 * kw + lane) * 16 : OOB;
    unsigned ta = (unsigned)(size_t)L + L_WO + (8 * r + hd) * 256 + (((2 * kw) ^ (hd | ((r & 1) << 2))) * 32) + jj * 8, tc = ta ^ 32u;
    unsigned aw = (unsigned)(size_t)L + L_OS + kw * 192 + (4 * c + r) * 2, ar = (unsigned)(size_t)L + L_OS + jj * 192 + r * 16, ah = (unsigned)(size_t)L + L_HS + jj * 64 + r * 16;
    unsigned apub = (unsigned)(size_t)(ct + C_PUB);
    KEEP(off); KEEP(offp); KEEP(ta); KEEP(tc); KEEP(aw); KEEP(ar); KEEP(ah); KEEP(t8); KEEP(apub);
    __builtin_amdgcn_s_setprio(2);
    pace_long(VSLOT(31) + T1 - 6);
    pace(VSLOT(31) + T1);
#pragma unroll 1
    for (unsigned n = 0; n < LSPIN; ++n) {
      unsigned v;
      asm volatile("ds_read_b32 %0, %1\n\ts_waitcnt lgkmcnt(0)" : "=v"(v) : "v"(apub) : "memory");
      if ((unsigned)__builtin_amdgcn_readfirstlane(v) != 0u) break;
    }
    v4u o0 = __builtin_amdgcn_raw_buffer_load_b128(rw, off, 0, AUX_SC1), o1 = __builtin_amdgcn_raw_buffer_load_b128(rw, off + 512, 0, AUX_SC1);
    SB();
    lds_wait_ge(ct + C_WRDY, 2u);
    f32x4 acc0, acc1;
    {
      v2u t[4]; v4u h0;
      asm volatile("ds_read_b128 %4, %7\n\tds_read_b64_tr_b16 %0, %5 offset:16384\n\tds_read_b64_tr_b16 %1, %5 offset:17408\n\t"
                   "ds_read_b64_tr_b16 %2, %6 offset:16384\n\tds_read_b64_tr_b16 %3, %6 offset:17408\n\ts_waitcnt lgkmcnt(0)"
                   : "=&v"(t[0]), "=&v"(t[1]), "=&v"(t[2]), "=&v"(t[3]), "=&v"(h0) : "v"(ta), "v"(tc), "v"(ah) : "memory");
      acc0 = mfma32(h0, v4u{t[0].x, t[0].y, t[1].x, t[1].y}, f32x4{0, 0, 0, 0});
      acc1 = mfma32(h0, v4u{t[2].x, t[2].y, t[3].x, t[3].y}, f32x4{0, 0, 0, 0});
      KEEP(acc0); KEEP(acc1);
    }
    SB();
    o0 = dec4(o0, t8); o1 = dec4(o1, t8);
    if (__builtin_expect(!__all(((or4(o0) | or4(o1)) & 0xffu) == 0u), 0)) {
#pragma unroll 1
      for (unsigned n = 0; n < GSPIN; ++n) {
        MEMFENCE();
        o0 = dec4(__builtin_amdgcn_raw_buffer_load_b128(rw, off, 0, AUX_SC1), t8); o1 = dec4(__builtin_amdgcn_raw_buffer_load_b128(rw, off + 512, 0, AUX_SC1), t8);
        if (__all(((or4(o0) | or4(o1)) & 0xffu) == 0u)) break;
      }
    }
    v2u t[8]; unsigned fl;
#define WO_READS "ds_read_b64_tr_b16 %0, %9\n\tds_read_b64_tr_b16 %1, %9 offset:1024\n\tds_read_b64_tr_b16 %2, %10\n\tds_read_b64_tr_b16 %3, %10 offset:1024\n\t" \
                 "ds_read_b64_tr_b16 %4, %9 offset:8192\n\tds_read_b64_tr_b16 %5, %9 offset:9216\n\tds_read_b64_tr_b16 %6, %10 offset:8192\n\tds_read_b64_tr_b16 %7, %10 offset:9216"
    asm volatile("ds_read_b32 %8, %11 offset:24\n\t" WO_READS
                 : "=&v"(t[0]), "=&v"(t[1]), "=&v"(t[2]), "=&v"(t[3]), "=&v"(t[4]), "=&v"(t[5]), "=&v"(t[6]), "=&v"(t[7]), "=&v"(fl) : "v"(ta), "v"(tc), "v"(apub) : "memory");
    SB();
    const f32x4 y = asf(o0) * w0 + asf(o1) * w1;
    const float ov = add16(add32(y[0], y[2]), add32(y[1], y[3]));
    const unsigned ob = pk2(ov, ov);
    v4u af0, af1;
    asm volatile("ds_write_b16 %11, %12\n\ts_waitcnt lgkmcnt(0)\n\ts_barrier\n\tds_read_b128 %0, %13\n\tds_read_b128 %1, %13 offset:64"
                 : "=&v"(af0), "=&v"(af1), "+v"(t[0]), "+v"(t[1]), "+v"(t[2]), "+v"(t[3]), "+v"(t[4]), "+v"(t[5]), "+v"(t[6]), "+v"(t[7]), "+v"(fl) : "v"(aw), "v"(ob), "v"(ar) : "memory");
    if (__builtin_expect(__builtin_amdgcn_readfirstlane(fl) != 2, 0))
      asm volatile(WO_READS "\n\ts_waitcnt lgkmcnt(0)"
                   : "=&v"(t[0]), "=&v"(t[1]), "=&v"(t[2]), "=&v"(t[3]), "=&v"(t[4]), "=&v"(t[5]), "=&v"(t[6]), "=&v"(t[7]), "+v"(fl) : "v"(ta), "v"(tc) : "memory");
    asm volatile("s_waitcnt lgkmcnt(0)" : "+v"(af0), "+v"(af1) :: "memory");
    acc0 = mfma32(af0, v4u{t[0].x, t[0].y, t[1].x, t[1].y}, acc0); acc1 = mfma32(af0, v4u{t[2].x, t[2].y, t[3].x, t[3].y}, acc1);
    acc0 = mfma32(af1, v4u{t[4].x, t[4].y, t[5].x, t[5].y}, acc0); acc1 = mfma32(af1, v4u{t[6].x, t[6].y, t[7].x, t[7].y}, acc1);
    __builtin_amdgcn_raw_buffer_store_b128(enc4(r == 0 ? acc0 : acc1, t8), rw, offp, 0, AUX_SC1);
    return;
  }

  const int sw = wave - 6;
  const __amdgpu_buffer_rsrc_t rv = rsrc(Vc + (size_t)pair * 1048576, 1048576);
  LAS char* kb = L + L_K + sw * 32768;
  const int col0 = grp * 64 + mem * 2;
  float resid = bflo(__builtin_amdgcn_raw_buffer_load_b16(rsrc(x, 4096), (r * 512 + col0 + (lane & 1)) * 2, 0, 0));
  unsigned tag = __builtin_amdgcn_raw_buffer_load_b32(rw, WS_EPOCH, 0, AUX_SC1) + 1u;
  unsigned t8 = tag & 0xffu;
  int offo = WS_O + ((hi2 * 32 + 4 * grp + hd) * 8 + lo3) * 512 + r * 64 + jj * 16;
  unsigned mk0 = jj == 0 ? 0x0000ffffu : jj == 1 ? 0xffff0000u : 0u, mk1 = jj == 2 ? 0x0000ffffu : jj == 3 ? 0xffff0000u : 0u;
  unsigned one = 1u, amrg = (unsigned)(size_t)(ct + C_MRG);
  unsigned amw = (unsigned)(size_t)kb + lane * 16, am0 = (unsigned)(size_t)(L + L_K) + 16384 + lane * 16, am2 = am0 + 65536;
  KEEP(amw); KEEP(am0); KEEP(am2);
  KEEP(resid); KEEP(tag); KEEP(t8); KEEP(offo); KEEP(mk0); KEEP(mk1); KEEP(one); KEEP(amrg);
  if (sw == 1) __builtin_amdgcn_raw_buffer_store_b64(v2u{tb, tag}, rw, (blk < 8 && lane == 0) ? WS_ST + blk * 8 : OOB, 0, AUX_SC1);
  int so[32];
#pragma unroll
  for (int i = 0; i < 32; ++i) { so[i] = KVOFF(i, sw); asm volatile("" : "+s"(so[i])); }
  asm volatile("" :: "s"(rv));
  if (sw == 2) {
    pace_long(tb + TPT);
    const int o1 = lane == 0 ? 0 : OOB;
    unsigned z = __builtin_amdgcn_raw_buffer_load_b32(rv, o1, so[0], AUX_NT);
    z |= __builtin_amdgcn_raw_buffer_load_b32(rsrc(wo, 4194304), o1, hh * 131072, AUX_NT);
    z |= __builtin_amdgcn_raw_buffer_load_b32(rsrc(wd, 1792 * 1024), o1, 28 * t2 * 1024, AUX_NT);
    z |= __builtin_amdgcn_raw_buffer_load_b32(rsrc(out, 4096), o1, grp * 128, AUX_NT);
    asm volatile("" :: "v"(z));
  }
  if (sw == 1) {
    pace_long(tb + TA);
    const v2u stv = __builtin_amdgcn_raw_buffer_load_b64(rw, WS_ST + (lane & 7) * 8, 0, AUX_SC1);
    int dl = stv.y == tag ? (int)(stv.x - tb) : 0;
    dl = min(max(dl, -250), 250);
    int sm = dl + (int)dppu<XOR1>((unsigned)dl); sm += (int)dppu<XOR2>((unsigned)sm); sm += (int)dppu<HMIR>((unsigned)sm);
    int mxd = max(dl, (int)dppu<XOR1>((unsigned)dl)); mxd = max(mxd, (int)dppu<XOR2>((unsigned)mxd)); mxd = max(mxd, (int)dppu<HMIR>((unsigned)mxd));
    lds_set(ct + C_ANC, tb + max(sm >> 3, mxd - ANC));
    lds_set(ct + C_ARDY, 1u);
  }
  GET_TV();
  pace_long(tv - 8);
  v4u v[32], P0, P1;
  f32x4 a0 = {0, 0, 0, 0}, a1 = a0;
#define VSTEP(i) do { \
    pace(VSLOT(i)); \
    v[i] = __builtin_amdgcn_raw_buffer_load_b128(rv, lane * 16, so[i], AUX_NT); \
    CAP(); \
    if ((i) == 31) __builtin_amdgcn_s_setprio(3); \
    SB(); \
    if ((i) == PV0) { lds_wait_ge(ct + C_PRDY + sw, 1u); P0 = *(LAS v4u*)(kb + lane * 16); P1 = *(LAS v4u*)(kb + 1024 + lane * 16); } \
    pv_range<pv_done((i) - 1), pv_done(i)>(v, P0, P1, mk0, mk1, a0, a1); \
    SB(); } while (0)
#define VSTEP4(i) VSTEP(i); VSTEP(i + 1); VSTEP(i + 2); VSTEP(i + 3)
  VSTEP4(0); VSTEP4(4); VSTEP4(8); VSTEP4(12); VSTEP4(16); VSTEP4(20); VSTEP4(24); VSTEP4(28);
  pv_range<pv_done(31), 32>(v, P0, P1, mk0, mk1, a0, a1);
  unsigned old;
  f32x4 t0[4], t1[4];
  asm volatile("s_nop 7\n\tds_write_b128 %9, %10 offset:16384\n\tds_write_b128 %9, %11 offset:17408\n\ts_mov_b64 exec, 1\n\tds_add_rtn_u32 %0, %12, %13\n\ts_mov_b64 exec, -1\n\t"
               "ds_read_b128 %1, %14\n\tds_read_b128 %2, %14 offset:32768\n\tds_read_b128 %3, %15\n\tds_read_b128 %4, %15 offset:32768\n\t"
               "ds_read_b128 %5, %14 offset:1024\n\tds_read_b128 %6, %14 offset:33792\n\tds_read_b128 %7, %15 offset:1024\n\tds_read_b128 %8, %15 offset:33792\n\t"
               "s_waitcnt lgkmcnt(4)"
               : "=&v"(old), "=&v"(t0[0]), "=&v"(t0[1]), "=&v"(t0[2]), "=&v"(t0[3]), "=&v"(t1[0]), "=&v"(t1[1]), "=&v"(t1[2]), "=&v"(t1[3])
               : "v"(amw), "v"(a0), "v"(a1), "v"(amrg), "v"(one), "v"(am0), "v"(am2) : "memory");
  if (__builtin_expect(__builtin_amdgcn_readfirstlane(old) == 3u, 1)) {
    __builtin_amdgcn_raw_buffer_store_b128(enc4((t0[0] + t0[1]) + (t0[2] + t0[3]), t8), rw, offo, 0, AUX_SC1);
    SB();
    asm volatile("s_waitcnt lgkmcnt(0)" : "+v"(t1[0]), "+v"(t1[1]), "+v"(t1[2]), "+v"(t1[3]) :: "memory");
    __builtin_amdgcn_raw_buffer_store_b128(enc4((t1[0] + t1[1]) + (t1[2] + t1[3]), t8), rw, offo + 256, 0, AUX_SC1);
    lds_set(ct + C_PUB, 1u);
  } else asm volatile("s_waitcnt lgkmcnt(0)" :: "v"(t1[0]), "v"(t1[1]), "v"(t1[2]), "v"(t1[3]) : "memory");
  if (sw != 0) return;
  int off = WS_P2 + ((col0 >> 7) * 64 + (lane >> 1)) * 2048 + ((col0 & 127) + (lane & 1)) * 16;
  int oo = c < 2 ? (r * 512 + col0 + c) * 2 : OOB;
  int oe = (blk == 0 && lane == 0) ? WS_EPOCH : OOB;
  const __amdgpu_buffer_rsrc_t rout = rsrc(out, 4096);
  KEEP(off); KEEP(oo); KEEP(oe);
  __builtin_amdgcn_s_barrier();
  { const unsigned ta_ = now32() + PD2, tb_ = VSLOT(31) + T2; pace((int)(ta_ - tb_) > 0 ? ta_ : tb_); }
  MEMFENCE();
  v4u pa = dec4(__builtin_amdgcn_raw_buffer_load_b128(rw, off, 0, AUX_SC1), t8), pb = dec4(__builtin_amdgcn_raw_buffer_load_b128(rw, off, 32 * 2048, AUX_SC1), t8);
  if (__builtin_expect(!__all(((or4(pa) | or4(pb)) & 0xffu) == 0u), 0)) {
#pragma unroll 1
    for (unsigned n = 0; n < GSPIN; ++n) {
      MEMFENCE();
      pa = dec4(__builtin_amdgcn_raw_buffer_load_b128(rw, off, 0, AUX_SC1), t8); pb = dec4(__builtin_amdgcn_raw_buffer_load_b128(rw, off, 32 * 2048, AUX_SC1), t8);
      if (__all(((or4(pa) | or4(pb)) & 0xffu) == 0u)) break;
    }
  }
  const f32x4 pp = asf(pa) + asf(pb);
  float f = add16(add32(pp[0], pp[2]), add32(pp[1], pp[3]));
  f += dpp<XOR2>(f); f += dpp<ROR4>(f); f += dpp<ROR8>(f);
  f += resid;
  __builtin_amdgcn_raw_buffer_store_b16((unsigned short)pk2(f, f), rout, oo, 0, 0);
  __builtin_amdgcn_raw_buffer_store_b32(tag, rw, oe, 0, 0);
}

extern "C" void kernel_launch(void* const* d_in, const int* in_sizes, int n_in, void* d_out, int out_size, void* d_ws, size_t ws_size, hipStream_t stream) {
  fused_decode<<<NWG, NTHR, 0, stream>>>((const char*)d_in[1], (const char*)d_in[0], (const char*)d_in[3], (const char*)d_in[6], (const char*)d_in[7], (char*)d_ws,
                                         (const char*)d_in[2], (char*)d_out, (const char*)d_in[4], (const char*)d_in[8]);
}
```

```cpp
#include <hip/hip_runtime.h>
#include <hip/hip_bf16.h>
#include <stdint.h>

#define LAS __attribute__((address_space(3)))
typedef unsigned v16u __attribute__((ext_vector_type(16)));
typedef unsigned v4u __attribute__((ext_vector_type(4)));
typedef unsigned v2u __attribute__((ext_vector_type(2)));
typedef float f32x4 __attribute__((ext_vector_type(4)));
typedef float f32x2 __attribute__((ext_vector_type(2)));
typedef short s16x4 __attribute__((ext_vector_type(4)));
typedef __bf16 bf16x8 __attribute__((ext_vector_type(8)));
typedef __bf16 bf16x2 __attribute__((ext_vector_type(2)));
typedef unsigned long long u64;

constexpr int TAUK16 = 230;
constexpr int TAUV16 = 228;
constexpr int KE = 5;
constexpr int TW = 70;
constexpr int WPACE16 = 90;
constexpr int TK1 = 181;
constexpr int TV0 = TK1 + (((32 - KE) * TAUK16) >> 4);
constexpr int TPT = 360;
constexpr int TA = TV0 - 130;
constexpr int TQ = TV0 - 30;
constexpr int TG = TV0 + 150;
constexpr int ANC1 = 28, ANC2 = 6;
constexpr int RAMP = 30;
constexpr int TWO = 35;
constexpr int WOPACE16 = 70;
constexpr int T1 = 134;
constexpr int T2 = 225;
constexpr int PD2 = 12;
constexpr int NCAP = 12;
constexpr int PV0 = 22, PVN = 3, PVLAG = 10;

constexpr int NWG = 256, NTHR = 640;
constexpr int AUX_NT = 2, AUX_SC1 = 16, RSRC_W3 = 0x00020000, OOB = 0x7fffffff;
constexpr int CODE_BYTES = 17152;
constexpr int WS_EPOCH = 0, WS_ST = 1024, WS_ML = 4096, WS_QP = WS_ML + 4 * 32 * 128, WS_GP = WS_QP + 4 * 4 * 4096 * 4,
              WS_O = WS_GP + 2 * 4 * 4 * 1792 * 4, WS_P2 = WS_O + 4 * 32 * 8 * 512, WS_END = WS_P2 + 4 * 64 * 2048;
constexpr int XS_WAVE = 16 * 80 + 1024;
constexpr int L_K = 0, L_WO = 131072, L_WD = L_WO + 16384, L_QS = L_WD + 8192, L_OS = L_QS + 1280, L_HS = L_OS + 768, L_XS = L_HS + 256,
              L_MX = L_XS + 2 * XS_WAVE, L_LX = L_MX + 64, L_CT = L_LX + 64, L_END = L_CT + 64;
enum { C_QRDY = 0, C_MAX = 1, C_PRDY = 2  , C_PCNT = 6, C_MRG = 7, C_PUB = 8, C_WRDY = 9, C_ANC = 10, C_ARDY = 11, C_X0 = 12, C_X1 = 13, C_ORDY = 14 };
static_assert(C_ORDY == C_WRDY + 5 && C_ORDY == C_PUB + 6, "immediate offsets in the asm statements below");
constexpr unsigned GSPIN = 1u << 18, LSPIN = 1u << 24;
constexpr float LOG2E = 1.4426950408889634f;

__device__ __forceinline__ float bflo(unsigned u) { return __builtin_bit_cast(float, u << 16); }
__device__ __forceinline__ float bfhi(unsigned u) { return __builtin_bit_cast(float, u & 0xffff0000u); }
__device__ __forceinline__ unsigned pk2(float lo, float hi) { f32x2 f = {lo, hi}; return __builtin_bit_cast(unsigned, __builtin_convertvector(f, bf16x2)); }
__device__ __forceinline__ float rbf(float f) { return bflo(pk2(f, f)); }
template <int CTRL> __device__ __forceinline__ float dpp(float x) { return __builtin_bit_cast(float, __builtin_amdgcn_mov_dpp(__builtin_bit_cast(int, x), CTRL, 0xf, 0xf, true)); }
template <int CTRL> __device__ __forceinline__ unsigned dppu(unsigned x) { return (unsigned)__builtin_amdgcn_mov_dpp((int)x, CTRL, 0xf, 0xf, true); }
constexpr int XOR1 = 0xB1, XOR2 = 0x4E, HMIR = 0x141, ROR8 = 0x128, ROR4 = 0x124;
template <int CTRL> __device__ __forceinline__ double dppd(double x) { const v2u u = __builtin_bit_cast(v2u, x); return __builtin_bit_cast(double, v2u{dppu<CTRL>(u.x), dppu<CTRL>(u.y)}); }
__device__ __forceinline__ double row16_sum(double x) { x += dppd<XOR1>(x); x += dppd<XOR2>(x); x += dppd<HMIR>(x); x += dppd<ROR8>(x); return x; }
__device__ __forceinline__ float add16(float a, float b) { auto s = __builtin_amdgcn_permlane16_swap(__float_as_uint(a), __float_as_uint(b), false, false); return __uint_as_float(s[0]) + __uint_as_float(s[1]); }
__device__ __forceinline__ float add32(float a, float b) { auto s = __builtin_amdgcn_permlane32_swap(__float_as_uint(a), __float_as_uint(b), false, false); return __uint_as_float(s[0]) + __uint_as_float(s[1]); }
__device__ __forceinline__ float x16_max(float x) { auto s = __builtin_amdgcn_permlane16_swap(__float_as_uint(x), __float_as_uint(x), false, false); return fmaxf(__uint_as_float(s[0]), __uint_as_float(s[1])); }
__device__ __forceinline__ float x32_max(float x) { auto s = __builtin_amdgcn_permlane32_swap(__float_as_uint(x), __float_as_uint(x), false, false); return fmaxf(__uint_as_float(s[0]), __uint_as_float(s[1])); }
__device__ __forceinline__ f32x4 mfma16(v2u a, v2u b, f32x4 c) { return __builtin_amdgcn_mfma_f32_16x16x16bf16_1k(__builtin_bit_cast(s16x4, a), __builtin_bit_cast(s16x4, b), c, 0, 0, 0); }
__device__ __forceinline__ f32x4 mfma32(v4u a, v4u b, f32x4 c) { return __builtin_amdgcn_mfma_f32_16x16x32_bf16(__builtin_bit_cast(bf16x8, a), __builtin_bit_cast(bf16x8, b), c, 0, 0, 0); }
__device__ __forceinline__ v4u enc4(f32x4 v, unsigned t8) { const v4u u = __builtin_bit_cast(v4u, v); return v4u{(u.x & ~0xffu) | t8, (u.y & ~0xffu) | t8, (u.z & ~0xffu) | t8, (u.w & ~0xffu) | t8}; }
__device__ __forceinline__ v4u dec4(v4u u, unsigned t8) { return v4u{u.x ^ t8, u.y ^ t8, u.z ^ t8, u.w ^ t8}; }
__device__ __forceinline__ unsigned or4(v4u u) { return (u.x | u.y | u.z) | u.w; }
__device__ __forceinline__ f32x4 asf(v4u u) { return __builtin_bit_cast(f32x4, u); }
__device__ __forceinline__ __amdgpu_buffer_rsrc_t rsrc(const void* p, int bytes) { return __builtin_amdgcn_make_buffer_rsrc((void*)p, 0, bytes, RSRC_W3); }
#define DRAIN_VM() asm volatile("s_waitcnt vmcnt(0)" ::: "memory")
#define DRAIN_LGKM() asm volatile("s_waitcnt lgkmcnt(0)" ::: "memory")
#define CAP() asm volatile("s_waitcnt vmcnt(%0)" :: "i"(NCAP - 1) : "memory")
#define MEMFENCE() asm volatile("" ::: "memory")
#define SB() __builtin_amdgcn_sched_barrier(0)
#define KEEP(x) asm volatile("" : "+v"(x))

__device__ __forceinline__ unsigned now32() { return (unsigned)__builtin_amdgcn_s_memrealtime(); }
__device__ __forceinline__ void pace(unsigned t) { while ((int)(now32() - t) < 0) {} }
__device__ __forceinline__ void pace_long(unsigned t) { while ((int)(now32() - t) < 0) __builtin_amdgcn_s_sleep(1); }
__device__ __forceinline__ unsigned lds_get(LAS unsigned* p) { unsigned v; asm volatile("ds_read_b32 %0, %1\n\ts_waitcnt lgkmcnt(0)" : "=v"(v) : "v"((unsigned)(size_t)p) : "memory"); return __builtin_amdgcn_readfirstlane(v); }
__device__ __forceinline__ void lds_wait_ge(LAS unsigned* p, unsigned want) {
#pragma unroll 1
  for (unsigned n = 0; n < LSPIN; ++n) {
    if (lds_get(p) >= want) break;
    __builtin_amdgcn_s_sleep(1);
  }
}
__device__ __forceinline__ void lds_set(LAS unsigned* p, unsigned v) { asm volatile("ds_write_b32 %0, %1" :: "v"((unsigned)(size_t)p), "v"(v) : "memory"); }
__device__ __forceinline__ void lds_add1(LAS unsigned* p, int lane) { if (lane == 0) asm volatile("ds_add_u32 %0, %1" :: "v"((unsigned)(size_t)p), "v"(1u) : "memory"); }

__device__ __forceinline__ int pos2col(int pos) { return 32 * ((pos >> 4) & 3) + 8 * (pos & 3) + 4 * (pos >> 6) + ((pos >> 2) & 3); }

__device__ __forceinline__ void ln_tile(const v4u (&xr)[4], const v4u (&w)[16], LAS char* xs, int lane, int hf, int rot, f32x4& d0, f32x4& d1) {
  float e[32];
#pragma unroll
  for (int k = 0; k < 4; ++k)
#pragma unroll
    for (int j = 0; j < 4; ++j) { e[8 * k + 2 * j] = bflo(xr[k][j]); e[8 * k + 2 * j + 1] = bfhi(xr[k][j]); }
  double s0 = 0., s1 = 0., q0 = 0., q1 = 0.;
#pragma unroll
  for (int k = 0; k < 16; ++k) { const double a = e[2 * k], b = e[2 * k + 1]; s0 += a; s1 += b; q0 = fma(a, a, q0); q1 = fma(b, b, q1); }
  const double md = row16_sum(s0 + s1) * (1. / 512.);
  const float mean = (float)md, var = rbf((float)fmax(row16_sum(q0 + q1) * (1. / 512.) - md * md, 0.));
  const float mb = rbf(mean), den = rbf(sqrtf(rbf(var + 1e-5f))), inv = 1.f / den;
  const int g = lane >> 4, c = lane & 15, bb = (lane >> 2) & 3, jj = lane & 3;
  if ((c >> 3) == hf) {
#pragma unroll
    for (int j = 0; j < 4; ++j) {
      const unsigned u = pk2(rbf(e[j] - mb) * inv, rbf(e[j + 4] - mb) * inv);
      *(LAS unsigned*)(xs + (g * 4 + j) * 80 + (c & 7) * 4) = u; *(LAS unsigned*)(xs + (g * 4 + j) * 80 + (c & 7) * 4 + 32) = u;
    }
  }
  DRAIN_LGKM();
  unsigned pr[8];
#pragma unroll
  for (int i = 0; i < 8; ++i) pr[i] = *(LAS unsigned*)(xs + (bb * 4 + g) * 80 + rot * 2 + 4 * i);
  SB();
  const unsigned z = 0x0c0c0c0cu;
  const unsigned sA0 = jj == 0 ? 0x0c0c0100u : jj == 1 ? 0x01000c0cu : z, sB0 = jj == 2 ? 0x0c0c0100u : jj == 3 ? 0x01000c0cu : z;
  const unsigned sA1 = jj == 0 ? 0x0c0c0302u : jj == 1 ? 0x03020c0cu : z, sB1 = jj == 2 ? 0x0c0c0302u : jj == 3 ? 0x03020c0cu : z;
  d0 = f32x4{0, 0, 0, 0}; d1 = d0;
#pragma unroll
  for (int i = 0; i < 16; ++i) {
    const unsigned p = pr[i >> 1];
    const v2u b = {__builtin_amdgcn_perm(p, p, (i & 1) ? sA1 : sA0), __builtin_amdgcn_perm(p, p, (i & 1) ? sB1 : sB0)};
    d0 = mfma16(v2u{w[i].x, w[i].y}, b, d0); d1 = mfma16(v2u{w[i].z, w[i].w}, b, d1);
  }
}

constexpr int pv_done(int i) { return i < PV0 ? 0 : (PVN * (i - PV0 + 1) < i + 1 - PVLAG ? PVN * (i - PV0 + 1) : i + 1 - PVLAG); }
template <int J> __device__ __forceinline__ void pv_step(const v4u& v, const v4u& P0, const v4u& P1, unsigned mk0, unsigned mk1, f32x4& a0, f32x4& a1) {
  const unsigned s = (J < 16 ? P0 : P1)[J & 3];
  const v2u p = {dppu<0x55 * ((J >> 2) & 3)>(s) & mk0, dppu<0x55 * ((J >> 2) & 3)>(s) & mk1};
  a0 = mfma16(v2u{v.x, v.y}, p, a0); a1 = mfma16(v2u{v.z, v.w}, p, a1);
}
template <int LO, int HI> __device__ __forceinline__ void pv_range(const v4u (&v)[32], const v4u& P0, const v4u& P1, unsigned mk0, unsigned mk1, f32x4& a0, f32x4& a1) {
  if constexpr (LO < HI) { pv_step<LO>(v[LO], P0, P1, mk0, mk1, a0, a1); pv_range<LO + 1, HI>(v, P0, P1, mk0, mk1, a0, a1); }
}

#define KVOFF(i, w) (((((i) + pair) & 31) << 15) + (((lo3 + (i)) & 1) << 10) + (((lo3 + (i)) & 6) << 12) + ((w) << 11))
#define VSLOT(s) (tv + (((s) * TAUV16) >> 4))
#define GET_TV() lds_wait_ge(ct + C_ARDY, 1u); const unsigned tv = lds_get(ct + C_ANC) + TV0

__global__ void __launch_bounds__(NTHR, 3)
fused_decode(const char* k_cache, const char* x_, const char* wq_, const char* w_gate, const char* w_up, char* ws_, const char* v_cache, char* out_, const char* wo_, const char* w_down) {
  const auto kargs = __builtin_amdgcn_kernarg_segment_ptr();
  u64 kc;
  asm volatile("s_load_dwordx2 %0, %1, 0x0" : "=&s"(kc) : "s"(kargs) : "memory");
  SB();
  __shared__ __attribute__((aligned(16))) char smem_[L_END];
  LAS char* L = (LAS char*)smem_;
  LAS unsigned* ct = (LAS unsigned*)(L + L_CT);
  const int lane = threadIdx.x & 63, wave = __builtin_amdgcn_readfirstlane(threadIdx.x >> 6), blk = blockIdx.x;
  const int grp = blk & 7, mem = blk >> 3, hi2 = mem >> 3, lo3 = mem & 7;
  const int hh = 4 * grp + hi2, pair = hi2 * 8 + grp;
  const int part = lo3 & 1, cq = lo3 >> 1, t2 = 2 * hh + part;
  if (wave == 0) {
    u64 pc; asm volatile("s_getpc_b64 %0" : "=s"(pc));
    __builtin_amdgcn_raw_ptr_buffer_load_lds(rsrc((void*)(pc & ~255ull), CODE_BYTES), (LAS void*)(L + L_WO), 16, lane * 16, mem * 1024, 0, 0);
  }
  int vk = (lane >> 4) * 256 + (((lane & 15) ^ (4 * (lane >> 4))) * 16);
  const bool isk = wave >= 2 && wave < 6;
  int pk0 = pair << 20, so0 = pk0 + KVOFF(0, wave - 2), lk0 = (int)(size_t)(L + L_K) + (wave - 2) * 32768;
  KEEP(vk); asm volatile("" : "+s"(so0), "+s"(pk0), "+s"(lk0));
  asm volatile("s_waitcnt lgkmcnt(0)" : "+s"(kc) :: "memory");
  const __amdgpu_buffer_rsrc_t rk = rsrc((const void*)kc, 33554432);
  if (isk) __builtin_amdgcn_raw_ptr_buffer_load_lds(rk, (LAS void*)(size_t)lk0, 16, vk, so0, 0, AUX_NT);
  SB();
  const unsigned tb = now32();
  if (wave == 9) { if (lane < 16) ct[lane] = 0u; }
  const int r = lane >> 4, c = lane & 15, hd = (lane >> 2) & 3, jj = lane & 3;
  DRAIN_LGKM();
  __builtin_amdgcn_s_barrier();
  if (isk) {
    const int kw = wave - 2;
    unsigned t16 = TAUK16;
#pragma unroll 1
    for (int j = 1; j < 32; ++j) {
      if (j == KE) t16 = TK1 * 16;
      const int vo_ = vk ^ ((j & 3) << 4), so_ = pk0 + KVOFF(j, kw);
      pace(tb + (t16 >> 4));
      __builtin_amdgcn_raw_ptr_buffer_load_lds(rk, (LAS void*)(size_t)(lk0 + j * 1024), 16, vo_, so_, 0, AUX_NT);
      CAP();
      t16 += TAUK16;
    }
  }
  v16u ka; v2u kz;
  asm volatile("s_load_dwordx16 %0, %2, 0x8\n\ts_load_dwordx2 %1, %2, 0x48\n\ts_waitcnt lgkmcnt(0)" : "=&s"(ka), "=&s"(kz) : "s"(kargs) : "memory");
#define P64(lo, hi) ((char*)(((u64)(hi) << 32) | (lo)))
  const char *x = P64(ka[0], ka[1]), *wq = P64(ka[2], ka[3]), *wg = P64(ka[4], ka[5]), *wu = P64(ka[6], ka[7]), *Vc = P64(ka[10], ka[11]), *wo = P64(ka[14], ka[15]), *wd = P64(kz[0], kz[1]);
  char *ws = P64(ka[8], ka[9]), *out = P64(ka[12], ka[13]);
  const __amdgpu_buffer_rsrc_t rw = rsrc(ws, WS_END);
  const int wd0 = ((28 * t2) & ~127) * 1024, wd1 = ((28 * t2 + 27) | 127) * 1024;

  if (wave < 2) {
    const __amdgpu_buffer_rsrc_t rx = rsrc(x, 4096);
    LAS char* xs = L + L_XS + wave * XS_WAVE;
    const bool tq = mem < 16, have = mem < 30, isq = wave == 0;
    const int tt = (mem - 16) * 8 + grp, cgg = tt >> 3, which = (tt >> 2) & 1;
    const int rs = tq ? mem >> 2 : tt & 3, pitch = tq ? 8192 : 3584, cb = tq ? (4 * grp + (mem & 3)) * 256 : cgg * 256, rot = tq ? 4 * rs + 2 * wave : 0;
    const __amdgpu_buffer_rsrc_t rq = rsrc(tq ? wq : which ? wu : wg, have ? 512 * pitch : 0);
    v4u xr[4], w[16];
    const unsigned epoch = __builtin_amdgcn_raw_buffer_load_b32(rw, WS_EPOCH, 0, AUX_SC1);
    SB();
#pragma unroll
    for (int i = 0; i < 16; ++i) {
      if (!(i & 1)) pace(tb + TW + ((i * WPACE16) >> 4));
      w[i] = __builtin_amdgcn_raw_buffer_load_b128(rq, r * pitch + c * 16, (rs * 128 + 64 * wave + 4 * ((i + rot) & 15)) * pitch + cb, AUX_NT);
      if (i == 5) {
#pragma unroll
        for (int k = 0; k < 4; ++k) xr[k] = __builtin_amdgcn_raw_buffer_load_b128(rx, r * 1024 + c * 16, ((rs + k) & 3) * 256, 0);
      }
      if (i & 1) SB();
    }
    f32x4 d0, d1;
    ln_tile(xr, w, xs, lane, wave, rot, d0, d1);
    *(LAS f32x4*)(xs + 16 * 80 + lane * 16) = isq ? d1 : d0;
    lds_set(ct + C_X0 + wave, 1u);
    lds_wait_ge(ct + C_X1 - wave, 1u);
    const f32x4 dx = *(LAS f32x4*)(L + L_XS + (1 - wave) * XS_WAVE + 16 * 80 + lane * 16);
    const f32x4 y = isq ? d0 + dx : dx + d1;
    const unsigned t8 = (epoch + 1u) & 0xffu;
    const int po = tq ? WS_QP + ((rs * 4 + hd) * 4096) * 4 + 2 * cb : WS_GP + (((which * 4 + rs) * 4 + hd) * 1792) * 4 + 2 * cb;
    const unsigned mp = tq ? 0xfu : 0xffu, mc = isq ? 0xfu : 0xffu, tc8 = t8 & mc;
    const v4u yr = ((__builtin_bit_cast(v4u, y) + ((mp + 1u) >> 1)) & ~mp) | (t8 & mp);
    __builtin_amdgcn_raw_buffer_store_b128(yr, rw, have ? po + wave * 256 + r * 64 + jj * 16 : OOB, 0, AUX_SC1);
    const __amdgpu_buffer_rsrc_t ro = rsrc(wo, 4194304), rd = rsrc(wd, 1792 * 1024);
    if (!isq) {
      pace_long(tb + TPT);
      unsigned z = __builtin_amdgcn_raw_buffer_load_b32(rsrc(Vc, 33554432), lane == 0 ? 0 : OOB, pair << 20, 0);
      z |= __builtin_amdgcn_raw_buffer_load_b32(ro, lane < 2 ? lane * 0x1fc00 : OOB, hh * 131072, 0);
      z |= __builtin_amdgcn_raw_buffer_load_b32(rd, lane == 0 ? wd0 : lane == 1 ? wd1 : OOB, 0, 0);
      z |= __builtin_amdgcn_raw_buffer_load_b32(rsrc(out, 4096), lane == 0 ? 0 : OOB, grp * 128, 0);
      asm volatile("" :: "v"(z));
    }
    const int go = isq ? WS_QP + (hi2 * 4096 + grp * 512) * 4 + lane * 16 : WS_GP + (r * 1792 + 28 * t2) * 4 + min(c, 6) * 16;
    const int gs = isq ? 4 * 4096 * 4 : 4 * 1792 * 4, gh = isq ? 1024 : 16 * 1792 * 4;
    pace_long(tb + (isq ? TQ : TG));
    v4u g[8];
#pragma unroll 1
    for (unsigned n = 0; n < GSPIN; ++n) {
      unsigned bad = 0;
#pragma unroll
      for (int k = 0; k < 8; ++k) g[k] = __builtin_amdgcn_raw_buffer_load_b128(rw, go, (k & 3) * gs + (k >> 2) * gh, AUX_SC1);
#pragma unroll
      for (int k = 0; k < 8; ++k) { g[k] = dec4(g[k], tc8); bad |= or4(g[k]); }
      if (__all((bad & mc) == 0u)) break;
      MEMFENCE();
      __builtin_amdgcn_s_sleep(8);
    }
    const f32x4 sa = (asf(g[0]) + asf(g[1])) + (asf(g[2]) + asf(g[3])), sb = (asf(g[4]) + asf(g[5])) + (asf(g[6]) + asf(g[7]));
    if (isq) {
#pragma unroll
      for (int u = 0; u < 2; ++u) {
        const f32x4 sq = u ? sb : sa;
        LAS char* q = L + L_QS + (2 * u + (lane >> 5)) * 320 + pos2col(4 * (lane & 31)) * 2;
        const unsigned a = pk2(sq.x, sq.y), d = pk2(sq.z, sq.w);
        *(LAS unsigned short*)(q) = (unsigned short)a; *(LAS unsigned short*)(q + 16) = (unsigned short)(a >> 16);
        *(LAS unsigned short*)(q + 32) = (unsigned short)d; *(LAS unsigned short*)(q + 48) = (unsigned short)(d >> 16);
      }
      lds_set(ct + C_QRDY, 1u);
    } else {
      v2u hv = {pk2(rbf(sa.x) * rbf(sb.x), rbf(sa.y) * rbf(sb.y)), pk2(rbf(sa.z) * rbf(sb.z), rbf(sa.w) * rbf(sb.w))};
      if (c >= 7) hv = v2u{0u, 0u};
      if (c < 8) *(LAS v2u*)(L + L_HS + r * 64 + c * 8) = hv;
    }
    const int wr = 2 * (hh & 3);
    int vo[2], vd[4];
#pragma unroll
    for (int p = 0; p < 2; ++p) { vo[p] = 8 * r * 1024 + ((c ^ (2 * (r | (((p + (wr >> 1)) & 1) << 2)))) * 16); KEEP(vo[p]); }
#pragma unroll
    for (int ii = 0; ii < 4; ++ii) {
      const int pos = 28 * t2 + 4 * (4 * wave + ii) + r, row = (pos & ~127) + pos2col(pos & 127);
      vd[ii] = 4 * wave + ii < 7 ? row * 1024 + ((c ^ (2 * (r | (((ii >> 1) & 1) << 2)))) * 16) : OOB;
      KEEP(vd[ii]);
    }
    DRAIN_LGKM();
    GET_TV();
    pace_long(VSLOT(31) + TWO - 10);
#pragma unroll
    for (int ii = 0; ii < 12; ++ii) {
      pace(VSLOT(31) + TWO + ((ii * WOPACE16) >> 4));
      if (ii < 4) __builtin_amdgcn_raw_ptr_buffer_load_lds(rd, (LAS void*)(L + L_WD + (4 * wave + ii) * 1024), 16, vd[ii], cq * 256, 0, AUX_NT);
      else { const int j = 8 * wave + ((ii - 4 + wr) & 7);
        __builtin_amdgcn_raw_ptr_buffer_load_lds(ro, (LAS void*)(L + L_WO + j * 1024), 16, vo[((ii - 4) >> 1) & 1], (hh * 128 + 32 * (j >> 2) + 4 * part + (j & 3)) * 1024 + cq * 256, 0, AUX_NT); }
      SB();
    }
    asm volatile("s_waitcnt vmcnt(8)\n\ts_mov_b64 exec, 1\n\tds_add_u32 %0, %1\n\ts_waitcnt vmcnt(0)\n\tds_add_u32 %0, %1 offset:20\n\ts_mov_b64 exec, -1"
                 :: "v"((unsigned)(size_t)(ct + C_WRDY)), "v"(1u) : "memory");
    return;
  }

  if (wave < 6) {
    const int kw = wave - 2;
    LAS char* kb = L + L_K + kw * 32768;
    unsigned tag = __builtin_amdgcn_raw_buffer_load_b32(rw, WS_EPOCH, 0, AUX_SC1);
    lds_wait_ge(ct + C_QRDY, 1u);
    DRAIN_VM();
    tag += 1u;
    unsigned t8 = tag & 0xffu;
    float sc[8];
    {
      v4u qf[4];
#pragma unroll
      for (int ks = 0; ks < 4; ++ks) qf[ks] = *(LAS v4u*)(L + L_QS + hd * 320 + ks * 64 + r * 16);
      f32x4 S[8];
      LAS char* kr = kb + (4 * (c & 3) + (c >> 2)) * 256;
      v4u kf[2][8];
#define KREAD(B, T0) _Pragma("unroll") for (int i = 0; i < 8; ++i) kf[B][i] = *(LAS v4u*)(kr + ((T0) + (i >> 2)) * 4096 + (((4 * (i & 3) + r) ^ c) * 16))
#define KMMA(B, T0) _Pragma("unroll") for (int i = 0; i < 8; ++i) { const int ks = i >> 1, T = (T0) + (i & 1); S[T] = mfma32(kf[B][4 * (i & 1) + ks], qf[ks], ks ? S[T] : f32x4{0, 0, 0, 0}); }
      KREAD(0, 0); KREAD(1, 2); SB();
      KMMA(0, 0); SB(); KREAD(0, 4); SB();
      KMMA(1, 2); SB(); KREAD(1, 6); SB();
      KMMA(0, 4); SB();
      KMMA(1, 6); SB();
#pragma unroll
      for (int U = 0; U < 2; ++U)
#pragma unroll
        for (int t = 0; t < 4; ++t) {
          const float lo = (jj & 1) ? S[4 * U + 1][t] : S[4 * U][t], hi = (jj & 1) ? S[4 * U + 3][t] : S[4 * U + 2][t];
          sc[4 * U + t] = (jj & 2) ? hi : lo;
        }
    }
    float mx = -INFINITY;
#pragma unroll
    for (int k = 0; k < 8; k += 2) {
      const unsigned u1 = pk2(sc[k], sc[k + 1]);
      const unsigned u2 = pk2(bflo(u1) * 0.08838834764831845f, bfhi(u1) * 0.08838834764831845f);
      sc[k] = bflo(u2); sc[k + 1] = bfhi(u2);
      mx = fmaxf(mx, fmaxf(sc[k], sc[k + 1]));
    }
    mx = fmaxf(mx, dpp<XOR1>(mx)); mx = fmaxf(mx, dpp<XOR2>(mx));
    mx = x32_max(x16_max(mx));
    if (lane < 16 && jj == 0) *(LAS float*)(L + L_MX + hd * 16 + kw * 4) = mx;
    lds_add1(ct + C_MAX, lane);
    lds_wait_ge(ct + C_MAX, 4u);
    const f32x4 m4 = *(LAS f32x4*)(L + L_MX + hd * 16);
    const float mw = fmaxf(fmaxf(m4.x, m4.y), fmaxf(m4.z, m4.w));
    float ls = 0.f;
    {
      v4u P[2];
#pragma unroll
      for (int k = 0; k < 8; ++k) { const float p = __builtin_amdgcn_exp2f((sc[k] - mw) * LOG2E); ls += p; P[k >> 2][k & 3] = pk2(p, p); }
      *(LAS v4u*)(kb + lane * 16) = P[0]; *(LAS v4u*)(kb + 1024 + lane * 16) = P[1];
      lds_set(ct + C_PRDY + kw, 1u);
    }
    ls += dpp<XOR1>(ls); ls += dpp<XOR2>(ls);
    ls = add16(ls, ls); ls = add32(ls, ls);
    if (lane < 16 && jj == 0) *(LAS float*)(L + L_LX + hd * 16 + kw * 4) = ls;
    lds_add1(ct + C_PCNT, lane);
    if (kw == 0) {
      lds_wait_ge(ct + C_PCNT, 4u);
      const f32x4 l4 = *(LAS f32x4*)(L + L_LX + hd * 16);
      const float lt = (l4.x + l4.y) + (l4.z + l4.w);
      __builtin_amdgcn_raw_buffer_store_b128(v4u{__float_as_uint(mw), tag, __float_as_uint(lt), tag}, rw,
                                             (lane < 16 && jj == 0) ? WS_ML + ((hi2 * 32 + 4 * grp + hd) * 8 + lo3) * 16 : OOB, 0, AUX_SC1);
    }
    float w0, w1;
    GET_TV();
    pace_long(VSLOT(32));
    {
      v4u a;
      const int offm = WS_ML + ((kw * 32 + hh) * 8 + (lane & 7)) * 16;
#pragma unroll 1
      for (unsigned n = 0; n < GSPIN; ++n) {
        a = __builtin_amdgcn_raw_buffer_load_b128(rw, offm, 0, AUX_SC1);
        if (__all(a.y == tag && a.w == tag)) break;
        MEMFENCE();
        __builtin_amdgcn_s_sleep(4);
      }
      const float m = __uint_as_float(a.x);
      float M = fmaxf(m, dpp<XOR1>(m)); M = fmaxf(M, dpp<XOR2>(M)); M = fmaxf(M, dpp<HMIR>(M));
      const float w = __builtin_amdgcn_exp2f((m - M) * LOG2E);
      float Ls = w * __uint_as_float(a.z); Ls += dpp<XOR1>(Ls); Ls += dpp<XOR2>(Ls); Ls += dpp<HMIR>(Ls);
      const int wn = __float_as_int(w / Ls);
      w0 = __int_as_float(__builtin_amdgcn_ds_bpermute(8 * r, wn)); w1 = __int_as_float(__builtin_amdgcn_ds_bpermute(8 * r + 4, wn));
      KEEP(w0); KEEP(w1);
    }
    int off = WS_O + ((kw * 32 + hh) * 8 + 2 * r) * 512 + part * 256 + c * 16;
    int offp = lane < 32 ? WS_P2 + (cq * 64 + t2) * 2048 + (32 * kw + lane) * 16 : OOB;
    unsigned ta = (unsigned)(size_t)L + L_WO + (8 * r + hd) * 256 + (((2 * kw) ^ (hd | ((r & 1) << 2))) * 32) + jj * 8, tc = ta ^ 32u;
    unsigned aw = (unsigned)(size_t)L + L_OS + kw * 192 + (4 * c + r) * 2, ar = (unsigned)(size_t)L + L_OS + jj * 192 + r * 16, ah = (unsigned)(size_t)L + L_HS + jj * 64 + r * 16;
    unsigned apub = (unsigned)(size_t)(ct + C_PUB);
    KEEP(off); KEEP(offp); KEEP(ta); KEEP(tc); KEEP(aw); KEEP(ar); KEEP(ah); KEEP(t8); KEEP(apub);
    __builtin_amdgcn_s_setprio(2);
    pace_long(VSLOT(31) + T1 - 6);
    pace(VSLOT(31) + T1);
#pragma unroll 1
    for (unsigned n = 0; n < LSPIN; ++n) {
      unsigned v;
      asm volatile("ds_read_b32 %0, %1\n\ts_waitcnt lgkmcnt(0)" : "=v"(v) : "v"(apub) : "memory");
      if ((unsigned)__builtin_amdgcn_readfirstlane(v) != 0u) break;
    }
    v4u o0 = __builtin_amdgcn_raw_buffer_load_b128(rw, off, 0, AUX_SC1), o1 = __builtin_amdgcn_raw_buffer_load_b128(rw, off + 512, 0, AUX_SC1);
    SB();
    lds_wait_ge(ct + C_WRDY, 2u);
    f32x4 acc0, acc1;
    {
      v2u t[4]; v4u h0;
      asm volatile("ds_read_b128 %4, %7\n\tds_read_b64_tr_b16 %0, %5 offset:16384\n\tds_read_b64_tr_b16 %1, %5 offset:17408\n\t"
                   "ds_read_b64_tr_b16 %2, %6 offset:16384\n\tds_read_b64_tr_b16 %3, %6 offset:17408\n\ts_waitcnt lgkmcnt(0)"
                   : "=&v"(t[0]), "=&v"(t[1]), "=&v"(t[2]), "=&v"(t[3]), "=&v"(h0) : "v"(ta), "v"(tc), "v"(ah) : "memory");
      acc0 = mfma32(h0, v4u{t[0].x, t[0].y, t[1].x, t[1].y}, f32x4{0, 0, 0, 0});
      acc1 = mfma32(h0, v4u{t[2].x, t[2].y, t[3].x, t[3].y}, f32x4{0, 0, 0, 0});
      KEEP(acc0); KEEP(acc1);
    }
    SB();
    o0 = dec4(o0, t8); o1 = dec4(o1, t8);
    if (__builtin_expect(!__all(((or4(o0) | or4(o1)) & 0xffu) == 0u), 0)) {
#pragma unroll 1
      for (unsigned n = 0; n < GSPIN; ++n) {
        MEMFENCE();
        o0 = dec4(__builtin_amdgcn_raw_buffer_load_b128(rw, off, 0, AUX_SC1), t8); o1 = dec4(__builtin_amdgcn_raw_buffer_load_b128(rw, off + 512, 0, AUX_SC1), t8);
        if (__all(((or4(o0) | or4(o1)) & 0xffu) == 0u)) break;
      }
    }
    v2u t[8]; unsigned fl;
#define WO_READS "ds_read_b64_tr_b16 %0, %9\n\tds_read_b64_tr_b16 %1, %9 offset:1024\n\tds_read_b64_tr_b16 %2, %10\n\tds_read_b64_tr_b16 %3, %10 offset:1024\n\t" \
                 "ds_read_b64_tr_b16 %4, %9 offset:8192\n\tds_read_b64_tr_b16 %5, %9 offset:9216\n\tds_read_b64_tr_b16 %6, %10 offset:8192\n\tds_read_b64_tr_b16 %7, %10 offset:9216"
    asm volatile("ds_read_b32 %8, %11 offset:24\n\t" WO_READS
                 : "=&v"(t[0]), "=&v"(t[1]), "=&v"(t[2]), "=&v"(t[3]), "=&v"(t[4]), "=&v"(t[5]), "=&v"(t[6]), "=&v"(t[7]), "=&v"(fl) : "v"(ta), "v"(tc), "v"(apub) : "memory");
    SB();
    const f32x4 y = __builtin_elementwise_fma(asf(o0), f32x4{w0, w0, w0, w0}, asf(o1) * w1);
    const float ov = add16(add32(y[0], y[2]), add32(y[1], y[3]));
    const unsigned ob = pk2(ov, ov);
    v4u af0, af1;
    unsigned fs;
    asm volatile("ds_write_b16 %14, %15\n\ts_waitcnt lgkmcnt(0)\n\tv_readfirstlane_b32 %13, %8\n\ts_barrier\n\tds_read_b128 %11, %16\n\tds_read_b128 %12, %16 offset:64\n\t"
                 "s_cmp_eq_u32 %13, 2\n\ts_cbranch_scc1 1f\n\t" WO_READS "\n1:\n\ts_waitcnt lgkmcnt(0)"
                 : "+v"(t[0]), "+v"(t[1]), "+v"(t[2]), "+v"(t[3]), "+v"(t[4]), "+v"(t[5]), "+v"(t[6]), "+v"(t[7]), "+v"(fl), "+v"(ta), "+v"(tc), "=&v"(af0), "=&v"(af1), "=&s"(fs)
                 : "v"(aw), "v"(ob), "v"(ar) : "memory", "scc");
    acc0 = mfma32(af0, v4u{t[0].x, t[0].y, t[1].x, t[1].y}, acc0); acc1 = mfma32(af0, v4u{t[2].x, t[2].y, t[3].x, t[3].y}, acc1);
    acc0 = mfma32(af1, v4u{t[4].x, t[4].y, t[5].x, t[5].y}, acc0); acc1 = mfma32(af1, v4u{t[6].x, t[6].y, t[7].x, t[7].y}, acc1);
    __builtin_amdgcn_raw_buffer_store_b128(enc4(r == 0 ? acc0 : acc1, t8), rw, offp, 0, AUX_SC1);
    return;
  }

  const int sw = wave - 6;
  const __amdgpu_buffer_rsrc_t rv = rsrc(Vc + (size_t)pair * 1048576, 1048576);
  LAS char* kb = L + L_K + sw * 32768;
  const int col0 = grp * 64 + mem * 2;
  if (sw == 3) {
    unsigned z0, z1, z2, z3, z4, z5, z6;
    asm volatile("s_load_dword %0, %7, 0x0\n\ts_load_dword %1, %8, 0x0\n\ts_load_dword %2, %9, 0x0\n\ts_load_dword %3, %10, 0x0\n\ts_load_dword %4, %10, 0x1fc00\n\t"
                 "s_load_dword %5, %11, 0x0\n\ts_load_dword %6, %12, 0x0\n\ts_waitcnt lgkmcnt(0)"
                 : "=&s"(z0), "=&s"(z1), "=&s"(z2), "=&s"(z3), "=&s"(z4), "=&s"(z5), "=&s"(z6)
                 : "s"(x + mem * 128), "s"(out + grp * 128), "s"(Vc + (size_t)pair * 1048576), "s"(wo + hh * 131072), "s"(wd + wd0), "s"(wd + wd1) : "memory");
  }
  float resid = 0.f;
  unsigned tag = __builtin_amdgcn_raw_buffer_load_b32(rw, WS_EPOCH, 0, AUX_SC1) + 1u;
  unsigned t8 = tag & 0xffu;
  int offo = WS_O + ((hi2 * 32 + 4 * grp + hd) * 8 + lo3) * 512 + r * 64 + jj * 16;
  unsigned mk0 = jj == 0 ? 0x0000ffffu : jj == 1 ? 0xffff0000u : 0u, mk1 = jj == 2 ? 0x0000ffffu : jj == 3 ? 0xffff0000u : 0u;
  unsigned one = 1u, amrg = (unsigned)(size_t)(ct + C_MRG);
  unsigned amw = (unsigned)(size_t)kb + lane * 16, am0 = (unsigned)(size_t)(L + L_K) + 16384 + lane * 16, am2 = am0 + 65536;
  KEEP(amw); KEEP(am0); KEEP(am2);
  KEEP(resid); KEEP(tag); KEEP(t8); KEEP(offo); KEEP(mk0); KEEP(mk1); KEEP(one); KEEP(amrg);
  if (sw == 1) __builtin_amdgcn_raw_buffer_store_b64(v2u{tb, tag}, rw, (blk < 8 && lane == 0) ? WS_ST + blk * 8 : OOB, 0, AUX_SC1);
  int so[32];
  const int vso = KVOFF(lane, sw);
#pragma unroll
  for (int i = 0; i < 32; ++i) { so[i] = __builtin_amdgcn_readlane(vso, i); asm volatile("" : "+s"(so[i])); }
  asm volatile("" :: "s"(rv));
  if (sw == 0) {
    pace_long(tb + 200u);
    resid = bflo(__builtin_amdgcn_raw_buffer_load_b16(rsrc(x, 4096), (r * 512 + col0 + (lane & 1)) * 2, 0, 0));
    KEEP(resid);
  }
  if (sw == 1) {
    pace_long(tb + TA);
    const v2u stv = __builtin_amdgcn_raw_buffer_load_b64(rw, WS_ST + (lane & 7) * 8, 0, AUX_SC1);
    int dl = stv.y == tag ? (int)(stv.x - tb) : 0;
    dl = min(max(dl, -250), 250);
    int sm = dl + (int)dppu<XOR1>((unsigned)dl); sm += (int)dppu<XOR2>((unsigned)sm); sm += (int)dppu<HMIR>((unsigned)sm);
    const int key = dl * 8 + (lane & 7);
    int k1 = max(key, (int)dppu<XOR1>((unsigned)key)); k1 = max(k1, (int)dppu<XOR2>((unsigned)k1)); k1 = max(k1, (int)dppu<HMIR>((unsigned)k1));
    int k2 = key == k1 ? -4096 : key;
    k2 = max(k2, (int)dppu<XOR1>((unsigned)k2)); k2 = max(k2, (int)dppu<XOR2>((unsigned)k2)); k2 = max(k2, (int)dppu<HMIR>((unsigned)k2));
    lds_set(ct + C_ANC, tb + max(sm >> 3, max((k1 >> 3) - ANC1, (k2 >> 3) - ANC2)));
    lds_set(ct + C_ARDY, 1u);
  }
  GET_TV();
  const unsigned tvo = tb + TV0;
  int taup = TAUV16 * 16 + ((int)(tv - tvo) * 256) / RAMP;
  asm volatile("" : "+s"(taup));
  pace_long(tvo - 8);
  v4u v[32], P0, P1;
  f32x4 a0 = {0, 0, 0, 0}, a1 = a0;
#define VSTEP(i) do { \
    pace((i) < RAMP ? tvo + (unsigned)(((i) * taup) >> 8) : VSLOT(i)); \
    v[i] = __builtin_amdgcn_raw_buffer_load_b128(rv, lane * 16, so[i], AUX_NT); \
    CAP(); \
    if ((i) == 31) __builtin_amdgcn_s_setprio(3); \
    SB(); \
    if ((i) == PV0) { lds_wait_ge(ct + C_PRDY + sw, 1u); P0 = *(LAS v4u*)(kb + lane * 16); P1 = *(LAS v4u*)(kb + 1024 + lane * 16); } \
    pv_range<pv_done((i) - 1), pv_done(i)>(v, P0, P1, mk0, mk1, a0, a1); \
    SB(); } while (0)
#define VSTEP4(i) VSTEP(i); VSTEP(i + 1); VSTEP(i + 2); VSTEP(i + 3)
  VSTEP4(0); VSTEP4(4); VSTEP4(8); VSTEP4(12); VSTEP4(16); VSTEP4(20); VSTEP4(24); VSTEP4(28);
  pv_range<pv_done(31), 32>(v, P0, P1, mk0, mk1, a0, a1);
  unsigned old;
  f32x4 t0[4], t1[4];
  asm volatile("s_nop 7\n\tds_write_b128 %9, %10 offset:16384\n\tds_write_b128 %9, %11 offset:17408\n\ts_mov_b64 exec, 1\n\tds_add_rtn_u32 %0, %12, %13\n\ts_mov_b64 exec, -1\n\t"
               "ds_read_b128 %1, %14\n\tds_read_b128 %2, %14 offset:32768\n\tds_read_b128 %3, %15\n\tds_read_b128 %4, %15 offset:32768\n\t"
               "ds_read_b128 %5, %14 offset:1024\n\tds_read_b128 %6, %14 offset:33792\n\tds_read_b128 %7, %15 offset:1024\n\tds_read_b128 %8, %15 offset:33792\n\t"
               "s_waitcnt lgkmcnt(4)"
               : "=&v"(old), "=&v"(t0[0]), "=&v"(t0[1]), "=&v"(t0[2]), "=&v"(t0[3]), "=&v"(t1[0]), "=&v"(t1[1]), "=&v"(t1[2]), "=&v"(t1[3])
               : "v"(amw), "v"(a0), "v"(a1), "v"(amrg), "v"(one), "v"(am0), "v"(am2) : "memory");
  if (__builtin_expect(__builtin_amdgcn_readfirstlane(old) == 3u, 1)) {
    __builtin_amdgcn_raw_buffer_store_b128(enc4((t0[0] + t0[1]) + (t0[2] + t0[3]), t8), rw, offo, 0, AUX_SC1);
    SB();
    asm volatile("s_waitcnt lgkmcnt(0)" : "+v"(t1[0]), "+v"(t1[1]), "+v"(t1[2]), "+v"(t1[3]) :: "memory");
    __builtin_amdgcn_raw_buffer_store_b128(enc4((t1[0] + t1[1]) + (t1[2] + t1[3]), t8), rw, offo + 256, 0, AUX_SC1);
    lds_set(ct + C_PUB, 1u);
  } else asm volatile("s_waitcnt lgkmcnt(0)" :: "v"(t1[0]), "v"(t1[1]), "v"(t1[2]), "v"(t1[3]) : "memory");
  if (sw != 0) return;
  int off = WS_P2 + ((col0 >> 7) * 64 + (lane >> 1)) * 2048 + ((col0 & 127) + (lane & 1)) * 16;
  int oo = c < 2 ? (r * 512 + col0 + c) * 2 : OOB;
  int oe = (blk == 0 && lane == 0) ? WS_EPOCH : OOB;
  const __amdgpu_buffer_rsrc_t rout = rsrc(out, 4096);
  KEEP(off); KEEP(oo); KEEP(oe);
  __builtin_amdgcn_s_barrier();
  { const unsigned ta_ = now32() + PD2, tb_ = VSLOT(31) + T2; pace((int)(ta_ - tb_) > 0 ? ta_ : tb_); }
  MEMFENCE();
  v4u pa = dec4(__builtin_amdgcn_raw_buffer_load_b128(rw, off, 0, AUX_SC1), t8), pb = dec4(__builtin_amdgcn_raw_buffer_load_b128(rw, off, 32 * 2048, AUX_SC1), t8);
  if (__builtin_expect(!__all(((or4(pa) | or4(pb)) & 0xffu) == 0u), 0)) {
#pragma unroll 1
    for (unsigned n = 0; n < GSPIN; ++n) {
      MEMFENCE();
      pa = dec4(__builtin_amdgcn_raw_buffer_load_b128(rw, off, 0, AUX_SC1), t8); pb = dec4(__builtin_amdgcn_raw_buffer_load_b128(rw, off, 32 * 2048, AUX_SC1), t8);
      if (__all(((or4(pa) | or4(pb)) & 0xffu) == 0u)) break;
    }
  }
  const f32x4 pp = asf(pa) + asf(pb);
  float f = add16(add32(pp[0], pp[2]), add32(pp[1], pp[3]));
  f += dpp<XOR2>(f); f += dpp<ROR4>(f); f += dpp<ROR8>(f);
  f += resid;
  __builtin_amdgcn_raw_buffer_store_b16((unsigned short)pk2(f, f), rout, oo, 0, 0);
  __builtin_amdgcn_raw_buffer_store_b32(tag, rw, oe, 0, 0);
}

extern "C" void kernel_launch(void* const* d_in, const int* in_sizes, int n_in, void* d_out, int out_size, void* d_ws, size_t ws_size, hipStream_t stream) {
  fused_decode<<<NWG, NTHR, 0, stream>>>((const char*)d_in[1], (const char*)d_in[0], (const char*)d_in[3], (const char*)d_in[6], (const char*)d_in[7], (char*)d_ws,
                                         (const char*)d_in[2], (char*)d_out, (const char*)d_in[4], (const char*)d_in[8]);
}
```
